```python
import torch

def _softmax(x, axis=-1):
    return torch.softmax(x, dim=axis)
def _gelu(x):
    return torch.nn.functional.gelu(x, approximate="tanh")
def _silu(x):
    return torch.nn.functional.silu(x)
def _sigmoid(x):
    return torch.sigmoid(x)
def _layernorm(x, axis=-1, eps=1e-5):
    m = x.mean(dim=axis, keepdim=True)
    v = x.var(dim=axis, keepdim=True, unbiased=False)
    return (x - m) / torch.sqrt(v + eps)
def _scatter_add(tgt, idx, src, axis=0):
    return tgt.clone().index_add_(axis, idx, src)
def _segment_sum(src, seg, n):
    out = torch.zeros((n,) + src.shape[1:], dtype=src.dtype)
    return out.index_add_(0, seg, src)
def _pad(x, pw):
    flat = tuple(v for pair in reversed(pw) for v in pair)
    return torch.nn.functional.pad(x, flat)

kernel_name = 'standalone__gqa_block_cached__near_ridge'

def setup_inputs():

    _w = (lambda a: (lambda t: t.to(torch.bfloat16) if t.is_floating_point() else t)(torch.as_tensor(a)))
    B, Sq, D = 16, 96, 576
    Hq, Hkv, Sk, Dh = 32, 16, 512, 128
    assert Hq % Hkv == 0, (Hq, Hkv)
    return {
        "x": _w((lambda *s: torch.randn(*s, dtype=torch.bfloat16) * 0.8)(B, Sq, D)),
        "k_cache": _w((lambda *s: torch.randn(*s, dtype=torch.bfloat16) * 0.8)(B, Hkv, Sk, Dh)),
        "v_cache": _w((lambda *s: torch.randn(*s, dtype=torch.bfloat16) * 0.05)(B, Hkv, Sk, Dh)),
        "wq": _w((lambda *s: torch.randn(*s, dtype=torch.bfloat16) * 0.8)(D, Hq * Dh) * (D ** -0.5)),
        "wo": _w((lambda *s: torch.randn(*s, dtype=torch.bfloat16) * 0.05)(Hq * Dh, D) * ((Hq * Dh) ** -0.5)),
    }

def reference(x, k_cache, v_cache, wq, wo):
    B, Sq, D = x.shape
    Hq, Dh = 32, 128
    Hkv = k_cache.shape[1]
    rep = Hq // Hkv
    q = (lambda x, *d: x.permute(*d))((x @ wq).reshape(B, Sq, Hq, Dh), 0, 2, 1, 3)
    k = (lambda x, n, axis: torch.repeat_interleave(x, n, dim=axis))(k_cache, rep, axis=1)
    v = (lambda x, n, axis: torch.repeat_interleave(x, n, dim=axis))(v_cache, rep, axis=1)
    scores = torch.einsum("bhqd,bhkd->bhqk", q, k) * (Dh ** -0.5)
    p = _softmax(scores, axis=-1)
    o = torch.einsum("bhqk,bhkd->bhqd", p, v)
    return (lambda x, *d: x.permute(*d))(o, 0, 2, 1, 3).reshape(B, Sq, Hq * Dh) @ wo
```

```cpp
#include <hip/hip_runtime.h>
#include <hip/hip_bf16.h>
typedef __hip_bfloat16 bf16;
constexpr int B=16,SQ=96,D=576,HQ=32,HKV=16,SK=512,DH=128;
__device__ inline float b2f(unsigned short u){ return __uint_as_float(((unsigned)u)<<16); }
__device__ inline unsigned short f2b(float f){ unsigned u=__float_as_uint(f); u = (u + 0x7FFF + ((u>>16)&1))>>16; return (unsigned short)u; }
__global__ void gemm_naive(const unsigned short* A, const unsigned short* W, unsigned short* C, int M, int N, int K){
  int n = blockIdx.x*blockDim.x+threadIdx.x; int m = blockIdx.y;
  if(n>=N) return;
  float acc=0;
  for(int k=0;k<K;k++) acc += b2f(A[(size_t)m*K+k])*b2f(W[(size_t)k*N+n]);
  C[(size_t)m*N+n]=f2b(acc);
}
__global__ void attn_naive(const unsigned short* Q, const unsigned short* Kc, const unsigned short* Vc, unsigned short* O){
  int q=blockIdx.x, h=blockIdx.y, b=blockIdx.z; int t=threadIdx.x;
  __shared__ float s[SK]; __shared__ float qs[DH]; __shared__ float red[128];
  qs[t]=b2f(Q[((size_t)b*SQ+q)*(HQ*DH)+h*DH+t]);
  __syncthreads();
  const unsigned short* Kb=Kc+((size_t)b*HKV+h/2)*SK*DH; const unsigned short* Vb=Vc+((size_t)b*HKV+h/2)*SK*DH;
  float mx=-1e30f;
  for(int k=t;k<SK;k+=128){ float a=0; for(int d=0;d<DH;d++) a+=qs[d]*b2f(Kb[k*DH+d]); a*=0.08838834764831845f; s[k]=a; mx=fmaxf(mx,a);}
  red[t]=mx; __syncthreads();
  for(int o=64;o>0;o>>=1){ if(t<o) red[t]=fmaxf(red[t],red[t+o]); __syncthreads(); }
  mx=red[0]; __syncthreads();
  float sm=0; for(int k=t;k<SK;k+=128){ float e=__expf(s[k]-mx); s[k]=e; sm+=e; }
  red[t]=sm; __syncthreads();
  for(int o=64;o>0;o>>=1){ if(t<o) red[t]+=red[t+o]; __syncthreads(); }
  sm=red[0];
  float a=0; for(int k=0;k<SK;k++) a+=s[k]*b2f(Vb[k*DH+t]);
  O[((size_t)b*SQ+q)*(HQ*DH)+h*DH+t]=f2b(a/sm);
}
extern "C" void kernel_launch(void* const* d_in, const int* in_sizes, int n_in, void* d_out, int out_size, void* d_ws, size_t ws_size, hipStream_t stream){
  const unsigned short* x=(const unsigned short*)d_in[0]; const unsigned short* kc=(const unsigned short*)d_in[1]; const unsigned short* vc=(const unsigned short*)d_in[2];
  const unsigned short* wq=(const unsigned short*)d_in[3]; const unsigned short* wo=(const unsigned short*)d_in[4];
  unsigned short* Q=(unsigned short*)d_ws; unsigned short* O=Q+(size_t)B*SQ*HQ*DH;
  gemm_naive<<<dim3(HQ*DH/256,B*SQ),256,0,stream>>>(x,wq,Q,B*SQ,HQ*DH,D);
  attn_naive<<<dim3(SQ,HQ,B),128,0,stream>>>(Q,kc,vc,O);
  gemm_naive<<<dim3((D+63)/64,B*SQ),64,0,stream>>>(O,wo,(unsigned short*)d_out,B*SQ,D,HQ*DH);
}
```

```cpp
#include <hip/hip_runtime.h>
#include <hip/hip_bf16.h>
typedef __hip_bfloat16 bf16;
constexpr int B=16,SQ=96,D=576,HQ=32,HKV=16,SK=512,DH=128;
__device__ inline float b2f(unsigned short u){ return __uint_as_float(((unsigned)u)<<16); }
__device__ inline unsigned short f2b(float f){ unsigned u=__float_as_uint(f); u = (u + 0x7FFF + ((u>>16)&1))>>16; return (unsigned short)u; }
__global__ void gemm_naive(const unsigned short* A, const unsigned short* W, unsigned short* C, int M, int N, int K){
  int n = blockIdx.x*blockDim.x+threadIdx.x; int m = blockIdx.y;
  if(n>=N) return;
  float acc=0;
  for(int k=0;k<K;k++) acc += b2f(A[(size_t)m*K+k])*b2f(W[(size_t)k*N+n]);
  C[(size_t)m*N+n]=f2b(acc);
}
__global__ void attn_naive(const unsigned short* Q, const unsigned short* Kc, const unsigned short* Vc, unsigned short* O){
  int q=blockIdx.x, h=blockIdx.y, b=blockIdx.z; int t=threadIdx.x;
  __shared__ float s[SK]; __shared__ float qs[DH]; __shared__ float red[128];
  qs[t]=b2f(Q[((size_t)b*SQ+q)*(HQ*DH)+h*DH+t]);
  __syncthreads();
  const unsigned short* Kb=Kc+((size_t)b*HKV+h/2)*SK*DH; const unsigned short* Vb=Vc+((size_t)b*HKV+h/2)*SK*DH;
  float mx=-1e30f;
  for(int k=t;k<SK;k+=128){ float a=0; for(int d=0;d<DH;d++) a+=qs[d]*b2f(Kb[k*DH+d]); a*=0.08838834764831845f; s[k]=a; mx=fmaxf(mx,a);}
  red[t]=mx; __syncthreads();
  for(int o=64;o>0;o>>=1){ if(t<o) red[t]=fmaxf(red[t],red[t+o]); __syncthreads(); }
  mx=red[0]; __syncthreads();
  float sm=0; for(int k=t;k<SK;k+=128){ float e=__expf(s[k]-mx); s[k]=e; sm+=e; }
  red[t]=sm; __syncthreads();
  for(int o=64;o>0;o>>=1){ if(t<o) red[t]+=red[t+o]; __syncthreads(); }
  sm=red[0];
  float a=0; for(int k=0;k<SK;k++) a+=s[k]*b2f(Vb[k*DH+t]);
  O[((size_t)b*SQ+q)*(HQ*DH)+h*DH+t]=f2b(a/sm);
}
#define MODE 3

typedef unsigned u32x4 __attribute__((ext_vector_type(4)));
template<int N> __device__ __forceinline__ void waitv(){ asm volatile("s_waitcnt vmcnt(%0)"::"i"(N):"memory"); }
template<int NT> __device__ __forceinline__ void dma4(__amdgpu_buffer_rsrc_t srd, unsigned voff, unsigned s0, unsigned s1, unsigned s2, unsigned s3, unsigned m0v){
  if constexpr(NT==0)
  asm volatile("s_mov_b32 m0, %6\n\ts_nop 0\n\tbuffer_load_dwordx4 %0, %1, %2 offen lds\n\tbuffer_load_dwordx4 %0, %1, %3 offen offset:1024 lds\n\tbuffer_load_dwordx4 %0, %1, %4 offen offset:2048 lds\n\tbuffer_load_dwordx4 %0, %1, %5 offen offset:3072 lds"
    :: "v"(voff), "s"(srd), "s"(s0), "s"(s1), "s"(s2), "s"(s3), "s"(m0v) : "memory");
  else
  asm volatile("s_mov_b32 m0, %6\n\ts_nop 0\n\tbuffer_load_dwordx4 %0, %1, %2 offen nt lds\n\tbuffer_load_dwordx4 %0, %1, %3 offen offset:1024 nt lds\n\tbuffer_load_dwordx4 %0, %1, %4 offen offset:2048 nt lds\n\tbuffer_load_dwordx4 %0, %1, %5 offen offset:3072 nt lds"
    :: "v"(voff), "s"(srd), "s"(s0), "s"(s1), "s"(s2), "s"(s3), "s"(m0v) : "memory");
}
constexpr int DW=4, DK=4;
__global__ __launch_bounds__(512,2) void probe(const char* W, const char* X, const char* Kc, const char* Vc){
  extern __shared__ __attribute__((aligned(16))) char lds[];
  int wid = __builtin_amdgcn_readfirstlane(threadIdx.x>>6); int lane=threadIdx.x&63;
  int bid=blockIdx.x; int xcd=bid&7, r=bid>>3; int kvh=xcd*2+(r&1), b=r>>1;
  unsigned long long t0=__builtin_amdgcn_s_memrealtime();
  unsigned ldsb=(unsigned)(size_t)lds;
  auto sW=__builtin_amdgcn_make_buffer_rsrc((void*)W,0,576*8192,0x00020000);
  auto sX=__builtin_amdgcn_make_buffer_rsrc((void*)(X+(size_t)b*110592),0,110592,0x00020000);
  auto sK=__builtin_amdgcn_make_buffer_rsrc((void*)(Kc+(size_t)(b*16+kvh)*131072),0,131072,0x00020000);
  auto sV=__builtin_amdgcn_make_buffer_rsrc((void*)(Vc+(size_t)(b*16+kvh)*131072),0,131072,0x00020000);
  unsigned vW=(lane>>5)*8192+kvh*512+(lane&31)*16, vL=lane*16;
  const int nload=4;
  if(MODE==0 && wid<nload){
    unsigned slot=0;
    int g0=(b*4)%72;
    for(int xg=wid;xg<27;xg+=nload){ unsigned q=xg*4096; dma4<0>(sX,vL,q,q,q,q,ldsb+(wid*DW+slot)*4096); slot=(slot+1)%DW; }
    for(int gi=wid;gi<72;gi+=nload){
      int g=gi+g0; if(g>=72) g-=72;
      unsigned s=g*65536;
      dma4<0>(sW,vW,s,s+16384-1024,s+32768-2048,s+49152-3072,ldsb+(wid*DW+slot)*4096); slot=(slot+1)%DW;
      waitv<(DW-1)*4>();
    }
    waitv<0>();
  }
  if((MODE==1||MODE==2) && wid<2){
    unsigned slot=0;
    for(int g=0;g<32;g++){
      unsigned q=g*4096;
      dma4<MODE==1?1:0>(wid?sV:sK,vL,q,q,q,q,ldsb+81920+(wid*DK+slot)*4096); slot=(slot+1)%DK;
      waitv<(DK-1)*4>();
    }
    waitv<0>();
  }
  __syncthreads();
  unsigned long long t1=__builtin_amdgcn_s_memrealtime();
  unsigned long long tend=t1+(t1-t0)*400ull;
  if(MODE==3) tend=t1;
  while(__builtin_amdgcn_s_memrealtime()<tend) __builtin_amdgcn_s_sleep(32);
}
extern "C" void kernel_launch(void* const* d_in, const int* in_sizes, int n_in, void* d_out, int out_size, void* d_ws, size_t ws_size, hipStream_t stream){
  const unsigned short* x=(const unsigned short*)d_in[0]; const unsigned short* kc=(const unsigned short*)d_in[1]; const unsigned short* vc=(const unsigned short*)d_in[2];
  const unsigned short* wq=(const unsigned short*)d_in[3]; const unsigned short* wo=(const unsigned short*)d_in[4];
  unsigned short* Q=(unsigned short*)d_ws; unsigned short* O=Q+(size_t)B*SQ*HQ*DH;
  hipFuncSetAttribute((const void*)probe, hipFuncAttributeMaxDynamicSharedMemorySize, 160*1024);
  probe<<<256,512,160*1024,stream>>>((const char*)wq,(const char*)x,(const char*)kc,(const char*)vc);
  gemm_naive<<<dim3(HQ*DH/256,B*SQ),256,0,stream>>>(x,wq,Q,B*SQ,HQ*DH,D);
  attn_naive<<<dim3(SQ,HQ,B),128,0,stream>>>(Q,kc,vc,O);
  gemm_naive<<<dim3((D+63)/64,B*SQ),64,0,stream>>>(O,wo,(unsigned short*)d_out,B*SQ,D,HQ*DH);
}
```

```cpp
#include <hip/hip_runtime.h>
#include <hip/hip_bf16.h>

__global__ __launch_bounds__(512, 2) void gqa_fused(const void* x, const void* k, const void* v, const void* wq, const void* wo, void* out, void* ws) {
  __shared__ __attribute__((aligned(16))) char lds[163840];
  const unsigned ldsb = (unsigned)(size_t)lds;
  const int wid = __builtin_amdgcn_readfirstlane(threadIdx.x >> 6);
  const int bid = blockIdx.x;
  const auto ka = __builtin_amdgcn_kernarg_segment_ptr();
  if (wid < 4) {
    asm volatile(
    "s_load_dwordx8 s[40:47], %[ka], 0x0\n\t"
    "s_load_dwordx2 s[48:49], %[ka], 0x20\n\t"
    "s_load_dwordx2 s[58:59], %[ka], 0x28\n\t"
    "s_load_dwordx2 s[56:57], %[ka], 0x30\n\t"
    "s_mov_b32 s54, %[wid]\n\t"
    "s_mov_b32 s30, %[bid]\n\t"
    "s_lshr_b32 s53, s30, 5\n\t"
    "s_bfe_u32 s31, s30, 0x20003\n\t"
    "s_bfe_u32 s51, s30, 0x20001\n\t"
    "s_and_b32 s50, s30, 1\n\t"
    "s_lshl_b32 s50, s50, 1\n\t"
    "s_xor_b32 s51, s51, s50\n\t"
    "s_lshl_b32 s51, s51, 2\n\t"
    "s_add_u32 s51, s51, s31\n\t"
    "s_and_b32 s50, s30, 1\n\t"
    "s_lshl_b32 s50, s50, 3\n\t"
    "s_add_u32 s50, s50, s53\n\t"
    "s_lshl_b32 s31, s31, 2\n\t"
    "s_lshr_b32 s52, 0x7420, s31\n\t"
    "s_and_b32 s52, s52, 15\n\t"
    "s_add_u32 s52, s52, s53\n\t"
    "s_cmp_ge_u32 s52, 9\n\t"
    "s_cselect_b32 s31, 9, 0\n\t"
    "s_sub_u32 s52, s52, s31\n\t"
    "s_waitcnt lgkmcnt(0)\n\t"
    "s_lshl_b32 s30, s52, 19\n\t"
    "s_lshl_b32 s31, s54, 17\n\t"
    "s_add_u32 s30, s30, s31\n\t"
    "s_lshl_b32 s31, s51, 9\n\t"
    "s_add_u32 s30, s30, s31\n\t"
    "s_load_dword s4, s[46:47], s30\n\t"
    "s_mul_i32 s30, s50, 110592\n\t"
    "s_load_dword s5, s[40:41], s30\n\t"
    "s_lshl_b32 s30, s50, 4\n\t"
    "s_add_u32 s30, s30, s51\n\t"
    "s_lshl_b32 s30, s30, 17\n\t"
    "s_load_dword s6, s[42:43], s30\n\t"
    "s_load_dword s7, s[44:45], s30\n\t"
    "s_setprio 2\n\t"
    "s_lshr_b32 s35, s54, 1\n\t"
    "s_and_b32 s36, s54, 1\n\t"
    "s_lshl_b32 s37, s35, 13\n\t"
    "s_mul_i32 s38, s36, 6144\n\t"
    "s_add_u32 s38, s38, 126976\n\t"
    "v_mbcnt_lo_u32_b32 v8, -1, 0\n\t"
    "v_mbcnt_hi_u32_b32 v8, -1, v8\n\t"
    "v_lshrrev_b32 v2, 5, v8\n\t"
    "v_lshlrev_b32 v2, 10, v2\n\t"
    "v_bfe_u32 v3, v8, 2, 3\n\t"
    "v_lshl_add_u32 v2, v3, 7, v2\n\t"
    "v_and_b32 v3, 3, v8\n\t"
    "v_lshl_add_u32 v2, v3, 3, v2\n\t"
    "v_add_u32 v2, s37, v2\n\t"
    "v_bfe_u32 v3, v8, 3, 2\n\t"
    "v_xor_b32 v5, 0, v3\n\t"
    "v_lshl_add_u32 v248, v5, 5, v2\n\t"
    "v_xor_b32 v5, 1, v3\n\t"
    "v_lshl_add_u32 v249, v5, 5, v2\n\t"
    "v_xor_b32 v5, 2, v3\n\t"
    "v_lshl_add_u32 v250, v5, 5, v2\n\t"
    "v_xor_b32 v5, 3, v3\n\t"
    "v_lshl_add_u32 v251, v5, 5, v2\n\t"
    "v_and_b32 v2, 15, v8\n\t"
    "v_lshrrev_b32 v3, 1, v2\n\t"
    "v_lshrrev_b32 v5, 4, v8\n\t"
    "v_xor_b32 v3, v3, v5\n\t"
    "v_lshlrev_b32 v2, 7, v2\n\t"
    "v_add_u32 v2, s38, v2\n\t"
    "v_lshl_add_u32 v252, v3, 4, v2\n\t"
    "v_xor_b32 v3, 4, v3\n\t"
    "v_lshl_add_u32 v253, v3, 4, v2\n\t"
    "v_bfe_u32 v2, v8, 1, 1\n\t"
    "v_bfe_u32 v3, v8, 4, 1\n\t"
    "v_xor_b32 v2, v2, v3\n\t"
    "v_bfe_u32 v3, v8, 2, 1\n\t"
    "v_bfe_u32 v5, v8, 5, 1\n\t"
    "v_xor_b32 v3, v3, v5\n\t"
    "v_lshl_add_u32 v2, v2, 1, v3\n\t"
    "v_bfe_u32 v3, v8, 3, 1\n\t"
    "v_lshl_add_u32 v2, v3, 2, v2\n\t"
    "v_and_b32 v3, 15, v8\n\t"
    "v_lshlrev_b32 v3, 7, v3\n\t"
    "v_add_u32 v3, 0x10000, v3\n\t"
    "v_lshl_add_u32 v254, v2, 4, v3\n\t"
    "v_xor_b32 v255, 64, v254\n\t"
    "s_mov_b32 s24, s56\n\t"
    "s_mov_b32 s25, s57\n\t"
    "s_and_b32 s25, s25, 0xffff\n\t"
    "s_mov_b32 s26, 1048576\n\t"
    "s_mov_b32 s27, 0x20000\n\t"
    "s_lshl_b32 s13, s50, 7\n\t"
    "s_xor_b32 s37, s37, 0x10000\n\t"
    "s_mov_b32 s28, 0x3e0293ee\n\t"
    "s_mov_b32 s29, 0x3e0293ee\n\t"
    "s_lshl_b32 s30, s50, 4\n\t"
    "s_add_u32 s30, s30, s51\n\t"
    "s_mul_i32 s30, s30, 110592\n\t"
    "s_add_u32 s20, s56, s30\n\t"
    "s_addc_u32 s21, s57, 0\n\t"
    "s_add_u32 s20, s20, 1048576\n\t"
    "s_addc_u32 s21, s21, 0\n\t"
    "s_and_b32 s21, s21, 0xffff\n\t"
    "s_mov_b32 s22, 110592\n\t"
    "s_mov_b32 s23, 0x20000\n\t"
    "s_mov_b32 s65, 0x4f800000\n\t"
    "s_mov_b32 s62, 1\n\t"
    "s_lshl_b32 s66, s50, 2\n\t"
    "s_add_u32 s66, s66, s54\n\t"
    "s_lshl_b32 s66, s66, 7\n\t"
    "s_add_u32 s66, s66, 270336\n\t"
    "s_add_u32 s66, s66, 8\n\t"
    "s_mul_i32 s67, s36, 12288\n\t"
    "s_mul_i32 s31, s35, 98304\n\t"
    "s_sub_u32 s30, 131072, s31\n\t"
    "s_add_u32 s31, s31, 32768\n\t"
    "s_add_u32 s68, s67, s31\n\t"
    "s_add_u32 s67, s67, s30\n\t"
    "s_mul_i32 s69, s54, 3072\n\t"
    "s_mul_i32 s14, s54, 3\n\t"
    "s_sub_u32 s15, s14, s51\n\t"
    "s_and_b32 s15, s15, 15\n\t"
    "s_add_u32 s63, s69, 131072\n\t"
    "s_mul_i32 s60, s35, 16448\n\t"
    "s_xor_b32 s60, s60, 0x18000\n\t"
    "s_xor_b32 s61, s60, 16384\n\t"
    "s_mov_b32 s9, 12288\n\t"
    "s_mov_b32 s10, 0\n\t"
    "s_mov_b32 s12, 0x10000\n\t"
    "v_mov_b64 v[0:1], 0\n\t"
    "v_mov_b64 v[2:3], 0\n\t"
    "v_mov_b64 v[4:5], 0\n\t"
    "v_mov_b64 v[6:7], 0\n\t"
    "v_mov_b64 v[8:9], 0\n\t"
    "v_mov_b64 v[10:11], 0\n\t"
    "v_mov_b64 v[12:13], 0\n\t"
    "v_mov_b64 v[14:15], 0\n\t"
    "v_mov_b64 v[16:17], 0\n\t"
    "v_mov_b64 v[18:19], 0\n\t"
    "v_mov_b64 v[20:21], 0\n\t"
    "v_mov_b64 v[22:23], 0\n\t"
    "v_mov_b64 v[24:25], 0\n\t"
    "v_mov_b64 v[26:27], 0\n\t"
    "v_mov_b64 v[28:29], 0\n\t"
    "v_mov_b64 v[30:31], 0\n\t"
    "v_mov_b64 v[32:33], 0\n\t"
    "v_mov_b64 v[34:35], 0\n\t"
    "v_mov_b64 v[36:37], 0\n\t"
    "v_mov_b64 v[38:39], 0\n\t"
    "v_mov_b64 v[40:41], 0\n\t"
    "v_mov_b64 v[42:43], 0\n\t"
    "v_mov_b64 v[44:45], 0\n\t"
    "v_mov_b64 v[46:47], 0\n\t"
    "v_mov_b64 v[48:49], 0\n\t"
    "v_mov_b64 v[50:51], 0\n\t"
    "v_mov_b64 v[52:53], 0\n\t"
    "v_mov_b64 v[54:55], 0\n\t"
    "v_mov_b64 v[56:57], 0\n\t"
    "v_mov_b64 v[58:59], 0\n\t"
    "v_mov_b64 v[60:61], 0\n\t"
    "v_mov_b64 v[62:63], 0\n\t"
    "v_mov_b64 v[64:65], 0\n\t"
    "v_mov_b64 v[66:67], 0\n\t"
    "v_mov_b64 v[68:69], 0\n\t"
    "v_mov_b64 v[70:71], 0\n\t"
    "v_mov_b64 v[72:73], 0\n\t"
    "v_mov_b64 v[74:75], 0\n\t"
    "v_mov_b64 v[76:77], 0\n\t"
    "v_mov_b64 v[78:79], 0\n\t"
    "v_mov_b64 v[80:81], 0\n\t"
    "v_mov_b64 v[82:83], 0\n\t"
    "v_mov_b64 v[84:85], 0\n\t"
    "v_mov_b64 v[86:87], 0\n\t"
    "v_mov_b64 v[88:89], 0\n\t"
    "v_mov_b64 v[90:91], 0\n\t"
    "v_mov_b64 v[92:93], 0\n\t"
    "v_mov_b64 v[94:95], 0\n\t"
    "s_waitcnt lgkmcnt(0)\n\t"
    "s_barrier\n\t"
    "ds_read_b128 v[160:163], v252 offset:0\n\t"
    "ds_read_b128 v[164:167], v252 offset:2048\n\t"
    "ds_read_b128 v[168:171], v252 offset:4096\n\t"
    "ds_read_b128 v[172:175], v253 offset:0\n\t"
    "ds_read_b128 v[176:179], v253 offset:2048\n\t"
    "ds_read_b128 v[180:183], v253 offset:4096\n\t"
    "ds_read_b64_tr_b16 v[96:97], v248 offset:32768\n\t"
    "ds_read_b64_tr_b16 v[98:99], v248 offset:34816\n\t"
    "ds_read_b64_tr_b16 v[100:101], v248 offset:36864\n\t"
    "ds_read_b64_tr_b16 v[102:103], v248 offset:38912\n\t"
    "ds_read_b64_tr_b16 v[104:105], v249 offset:32768\n\t"
    "ds_read_b64_tr_b16 v[106:107], v249 offset:34816\n\t"
    "ds_read_b64_tr_b16 v[108:109], v249 offset:36864\n\t"
    "ds_read_b64_tr_b16 v[110:111], v249 offset:38912\n\t"
    "ds_read_b64_tr_b16 v[112:113], v250 offset:32768\n\t"
    "ds_read_b64_tr_b16 v[114:115], v250 offset:34816\n\t"
    "ds_read_b64_tr_b16 v[116:117], v250 offset:36864\n\t"
    "ds_read_b64_tr_b16 v[118:119], v250 offset:38912\n\t"
    "ds_read_b64_tr_b16 v[120:121], v251 offset:32768\n\t"
    "ds_read_b64_tr_b16 v[122:123], v251 offset:34816\n\t"
    "ds_read_b64_tr_b16 v[124:125], v251 offset:36864\n\t"
    "ds_read_b64_tr_b16 v[126:127], v251 offset:38912\n\t"
    "s_mov_b32 s8, 3\n\t"
    "gqa_p1_0:\n\t"
    "s_waitcnt lgkmcnt(0)\n\t"
    "s_barrier\n\t"
    "v_mfma_f32_16x16x32_bf16 v[0:3], v[96:99], v[160:163], v[0:3]\n\t"
    "ds_read_b64_tr_b16 v[128:129], v248 offset:49152\n\t"
    "ds_read_b64_tr_b16 v[130:131], v248 offset:51200\n\t"
    "v_mfma_f32_16x16x32_bf16 v[0:3], v[100:103], v[172:175], v[0:3]\n\t"
    "ds_read_b64_tr_b16 v[132:133], v248 offset:53248\n\t"
    "v_mfma_f32_16x16x32_bf16 v[4:7], v[96:99], v[164:167], v[4:7]\n\t"
    "ds_read_b64_tr_b16 v[134:135], v248 offset:55296\n\t"
    "v_mfma_f32_16x16x32_bf16 v[4:7], v[100:103], v[176:179], v[4:7]\n\t"
    "ds_read_b64_tr_b16 v[136:137], v249 offset:49152\n\t"
    "v_mfma_f32_16x16x32_bf16 v[8:11], v[96:99], v[168:171], v[8:11]\n\t"
    "ds_read_b64_tr_b16 v[138:139], v249 offset:51200\n\t"
    "v_mfma_f32_16x16x32_bf16 v[8:11], v[100:103], v[180:183], v[8:11]\n\t"
    "ds_read_b64_tr_b16 v[140:141], v249 offset:53248\n\t"
    "v_mfma_f32_16x16x32_bf16 v[12:15], v[104:107], v[160:163], v[12:15]\n\t"
    "ds_read_b64_tr_b16 v[142:143], v249 offset:55296\n\t"
    "v_mfma_f32_16x16x32_bf16 v[12:15], v[108:111], v[172:175], v[12:15]\n\t"
    "ds_read_b64_tr_b16 v[144:145], v250 offset:49152\n\t"
    "v_mfma_f32_16x16x32_bf16 v[16:19], v[104:107], v[164:167], v[16:19]\n\t"
    "ds_read_b64_tr_b16 v[146:147], v250 offset:51200\n\t"
    "v_mfma_f32_16x16x32_bf16 v[16:19], v[108:111], v[176:179], v[16:19]\n\t"
    "ds_read_b64_tr_b16 v[148:149], v250 offset:53248\n\t"
    "v_mfma_f32_16x16x32_bf16 v[20:23], v[104:107], v[168:171], v[20:23]\n\t"
    "ds_read_b64_tr_b16 v[150:151], v250 offset:55296\n\t"
    "v_mfma_f32_16x16x32_bf16 v[20:23], v[108:111], v[180:183], v[20:23]\n\t"
    "ds_read_b64_tr_b16 v[152:153], v251 offset:49152\n\t"
    "v_mfma_f32_16x16x32_bf16 v[24:27], v[112:115], v[160:163], v[24:27]\n\t"
    "ds_read_b64_tr_b16 v[154:155], v251 offset:51200\n\t"
    "v_mfma_f32_16x16x32_bf16 v[24:27], v[116:119], v[172:175], v[24:27]\n\t"
    "ds_read_b64_tr_b16 v[156:157], v251 offset:53248\n\t"
    "v_mfma_f32_16x16x32_bf16 v[28:31], v[112:115], v[164:167], v[28:31]\n\t"
    "ds_read_b64_tr_b16 v[158:159], v251 offset:55296\n\t"
    "v_mfma_f32_16x16x32_bf16 v[28:31], v[116:119], v[176:179], v[28:31]\n\t"
    "s_add_u32 s10, s10, 12288\n\t"
    "s_cmp_eq_u32 s10, 36864\n\t"
    "v_mfma_f32_16x16x32_bf16 v[32:35], v[112:115], v[168:171], v[32:35]\n\t"
    "s_cselect_b32 s11, -24576, s9\n\t"
    "s_cselect_b32 s10, 0, s10\n\t"
    "v_mfma_f32_16x16x32_bf16 v[32:35], v[116:119], v[180:183], v[32:35]\n\t"
    "v_add_u32 v252, s11, v252\n\t"
    "v_add_u32 v253, s11, v253\n\t"
    "v_mfma_f32_16x16x32_bf16 v[36:39], v[120:123], v[160:163], v[36:39]\n\t"
    "v_mfma_f32_16x16x32_bf16 v[36:39], v[124:127], v[172:175], v[36:39]\n\t"
    "v_mfma_f32_16x16x32_bf16 v[40:43], v[120:123], v[164:167], v[40:43]\n\t"
    "v_mfma_f32_16x16x32_bf16 v[40:43], v[124:127], v[176:179], v[40:43]\n\t"
    "v_mfma_f32_16x16x32_bf16 v[44:47], v[120:123], v[168:171], v[44:47]\n\t"
    "v_mfma_f32_16x16x32_bf16 v[44:47], v[124:127], v[180:183], v[44:47]\n\t"
    "s_waitcnt lgkmcnt(0)\n\t"
    "s_barrier\n\t"
    "v_mfma_f32_16x16x32_bf16 v[48:51], v[128:131], v[160:163], v[48:51]\n\t"
    "ds_read_b128 v[184:187], v252 offset:0\n\t"
    "ds_read_b128 v[188:191], v252 offset:2048\n\t"
    "v_mfma_f32_16x16x32_bf16 v[48:51], v[132:135], v[172:175], v[48:51]\n\t"
    "ds_read_b128 v[192:195], v252 offset:4096\n\t"
    "v_mfma_f32_16x16x32_bf16 v[52:55], v[128:131], v[164:167], v[52:55]\n\t"
    "ds_read_b128 v[196:199], v253 offset:0\n\t"
    "ds_read_b128 v[200:203], v253 offset:2048\n\t"
    "v_mfma_f32_16x16x32_bf16 v[52:55], v[132:135], v[176:179], v[52:55]\n\t"
    "ds_read_b128 v[204:207], v253 offset:4096\n\t"
    "v_mfma_f32_16x16x32_bf16 v[56:59], v[128:131], v[168:171], v[56:59]\n\t"
    "ds_read_b64_tr_b16 v[96:97], v248 offset:0\n\t"
    "ds_read_b64_tr_b16 v[98:99], v248 offset:2048\n\t"
    "v_mfma_f32_16x16x32_bf16 v[56:59], v[132:135], v[180:183], v[56:59]\n\t"
    "ds_read_b64_tr_b16 v[100:101], v248 offset:4096\n\t"
    "v_mfma_f32_16x16x32_bf16 v[60:63], v[136:139], v[160:163], v[60:63]\n\t"
    "ds_read_b64_tr_b16 v[102:103], v248 offset:6144\n\t"
    "ds_read_b64_tr_b16 v[104:105], v249 offset:0\n\t"
    "v_mfma_f32_16x16x32_bf16 v[60:63], v[140:143], v[172:175], v[60:63]\n\t"
    "ds_read_b64_tr_b16 v[106:107], v249 offset:2048\n\t"
    "v_mfma_f32_16x16x32_bf16 v[64:67], v[136:139], v[164:167], v[64:67]\n\t"
    "ds_read_b64_tr_b16 v[108:109], v249 offset:4096\n\t"
    "ds_read_b64_tr_b16 v[110:111], v249 offset:6144\n\t"
    "v_mfma_f32_16x16x32_bf16 v[64:67], v[140:143], v[176:179], v[64:67]\n\t"
    "ds_read_b64_tr_b16 v[112:113], v250 offset:0\n\t"
    "v_mfma_f32_16x16x32_bf16 v[68:71], v[136:139], v[168:171], v[68:71]\n\t"
    "ds_read_b64_tr_b16 v[114:115], v250 offset:2048\n\t"
    "ds_read_b64_tr_b16 v[116:117], v250 offset:4096\n\t"
    "v_mfma_f32_16x16x32_bf16 v[68:71], v[140:143], v[180:183], v[68:71]\n\t"
    "ds_read_b64_tr_b16 v[118:119], v250 offset:6144\n\t"
    "v_mfma_f32_16x16x32_bf16 v[72:75], v[144:147], v[160:163], v[72:75]\n\t"
    "ds_read_b64_tr_b16 v[120:121], v251 offset:0\n\t"
    "ds_read_b64_tr_b16 v[122:123], v251 offset:2048\n\t"
    "v_mfma_f32_16x16x32_bf16 v[72:75], v[148:151], v[172:175], v[72:75]\n\t"
    "ds_read_b64_tr_b16 v[124:125], v251 offset:4096\n\t"
    "v_mfma_f32_16x16x32_bf16 v[76:79], v[144:147], v[164:167], v[76:79]\n\t"
    "ds_read_b64_tr_b16 v[126:127], v251 offset:6144\n\t"
    "v_mfma_f32_16x16x32_bf16 v[76:79], v[148:151], v[176:179], v[76:79]\n\t"
    "v_mfma_f32_16x16x32_bf16 v[80:83], v[144:147], v[168:171], v[80:83]\n\t"
    "v_mfma_f32_16x16x32_bf16 v[80:83], v[148:151], v[180:183], v[80:83]\n\t"
    "v_mfma_f32_16x16x32_bf16 v[84:87], v[152:155], v[160:163], v[84:87]\n\t"
    "v_mfma_f32_16x16x32_bf16 v[84:87], v[156:159], v[172:175], v[84:87]\n\t"
    "v_mfma_f32_16x16x32_bf16 v[88:91], v[152:155], v[164:167], v[88:91]\n\t"
    "v_mfma_f32_16x16x32_bf16 v[88:91], v[156:159], v[176:179], v[88:91]\n\t"
    "v_mfma_f32_16x16x32_bf16 v[92:95], v[152:155], v[168:171], v[92:95]\n\t"
    "v_mfma_f32_16x16x32_bf16 v[92:95], v[156:159], v[180:183], v[92:95]\n\t"
    "s_waitcnt lgkmcnt(0)\n\t"
    "s_barrier\n\t"
    "v_mfma_f32_16x16x32_bf16 v[0:3], v[96:99], v[184:187], v[0:3]\n\t"
    "ds_read_b64_tr_b16 v[128:129], v248 offset:16384\n\t"
    "ds_read_b64_tr_b16 v[130:131], v248 offset:18432\n\t"
    "v_mfma_f32_16x16x32_bf16 v[0:3], v[100:103], v[196:199], v[0:3]\n\t"
    "ds_read_b64_tr_b16 v[132:133], v248 offset:20480\n\t"
    "v_mfma_f32_16x16x32_bf16 v[4:7], v[96:99], v[188:191], v[4:7]\n\t"
    "ds_read_b64_tr_b16 v[134:135], v248 offset:22528\n\t"
    "v_mfma_f32_16x16x32_bf16 v[4:7], v[100:103], v[200:203], v[4:7]\n\t"
    "ds_read_b64_tr_b16 v[136:137], v249 offset:16384\n\t"
    "v_mfma_f32_16x16x32_bf16 v[8:11], v[96:99], v[192:195], v[8:11]\n\t"
    "ds_read_b64_tr_b16 v[138:139], v249 offset:18432\n\t"
    "v_mfma_f32_16x16x32_bf16 v[8:11], v[100:103], v[204:207], v[8:11]\n\t"
    "ds_read_b64_tr_b16 v[140:141], v249 offset:20480\n\t"
    "v_mfma_f32_16x16x32_bf16 v[12:15], v[104:107], v[184:187], v[12:15]\n\t"
    "ds_read_b64_tr_b16 v[142:143], v249 offset:22528\n\t"
    "v_mfma_f32_16x16x32_bf16 v[12:15], v[108:111], v[196:199], v[12:15]\n\t"
    "ds_read_b64_tr_b16 v[144:145], v250 offset:16384\n\t"
    "v_mfma_f32_16x16x32_bf16 v[16:19], v[104:107], v[188:191], v[16:19]\n\t"
    "ds_read_b64_tr_b16 v[146:147], v250 offset:18432\n\t"
    "v_mfma_f32_16x16x32_bf16 v[16:19], v[108:111], v[200:203], v[16:19]\n\t"
    "ds_read_b64_tr_b16 v[148:149], v250 offset:20480\n\t"
    "v_mfma_f32_16x16x32_bf16 v[20:23], v[104:107], v[192:195], v[20:23]\n\t"
    "ds_read_b64_tr_b16 v[150:151], v250 offset:22528\n\t"
    "v_mfma_f32_16x16x32_bf16 v[20:23], v[108:111], v[204:207], v[20:23]\n\t"
    "ds_read_b64_tr_b16 v[152:153], v251 offset:16384\n\t"
    "v_mfma_f32_16x16x32_bf16 v[24:27], v[112:115], v[184:187], v[24:27]\n\t"
    "ds_read_b64_tr_b16 v[154:155], v251 offset:18432\n\t"
    "v_mfma_f32_16x16x32_bf16 v[24:27], v[116:119], v[196:199], v[24:27]\n\t"
    "ds_read_b64_tr_b16 v[156:157], v251 offset:20480\n\t"
    "v_mfma_f32_16x16x32_bf16 v[28:31], v[112:115], v[188:191], v[28:31]\n\t"
    "ds_read_b64_tr_b16 v[158:159], v251 offset:22528\n\t"
    "v_mfma_f32_16x16x32_bf16 v[28:31], v[116:119], v[200:203], v[28:31]\n\t"
    "s_add_u32 s10, s10, 12288\n\t"
    "s_cmp_eq_u32 s10, 36864\n\t"
    "v_mfma_f32_16x16x32_bf16 v[32:35], v[112:115], v[192:195], v[32:35]\n\t"
    "s_cselect_b32 s11, -24576, s9\n\t"
    "s_cselect_b32 s10, 0, s10\n\t"
    "v_mfma_f32_16x16x32_bf16 v[32:35], v[116:119], v[204:207], v[32:35]\n\t"
    "v_add_u32 v252, s11, v252\n\t"
    "v_add_u32 v253, s11, v253\n\t"
    "v_mfma_f32_16x16x32_bf16 v[36:39], v[120:123], v[184:187], v[36:39]\n\t"
    "v_mfma_f32_16x16x32_bf16 v[36:39], v[124:127], v[196:199], v[36:39]\n\t"
    "v_mfma_f32_16x16x32_bf16 v[40:43], v[120:123], v[188:191], v[40:43]\n\t"
    "v_mfma_f32_16x16x32_bf16 v[40:43], v[124:127], v[200:203], v[40:43]\n\t"
    "v_mfma_f32_16x16x32_bf16 v[44:47], v[120:123], v[192:195], v[44:47]\n\t"
    "v_mfma_f32_16x16x32_bf16 v[44:47], v[124:127], v[204:207], v[44:47]\n\t"
    "s_waitcnt lgkmcnt(0)\n\t"
    "s_barrier\n\t"
    "v_mfma_f32_16x16x32_bf16 v[48:51], v[128:131], v[184:187], v[48:51]\n\t"
    "ds_read_b128 v[160:163], v252 offset:0\n\t"
    "ds_read_b128 v[164:167], v252 offset:2048\n\t"
    "v_mfma_f32_16x16x32_bf16 v[48:51], v[132:135], v[196:199], v[48:51]\n\t"
    "ds_read_b128 v[168:171], v252 offset:4096\n\t"
    "v_mfma_f32_16x16x32_bf16 v[52:55], v[128:131], v[188:191], v[52:55]\n\t"
    "ds_read_b128 v[172:175], v253 offset:0\n\t"
    "ds_read_b128 v[176:179], v253 offset:2048\n\t"
    "v_mfma_f32_16x16x32_bf16 v[52:55], v[132:135], v[200:203], v[52:55]\n\t"
    "ds_read_b128 v[180:183], v253 offset:4096\n\t"
    "v_mfma_f32_16x16x32_bf16 v[56:59], v[128:131], v[192:195], v[56:59]\n\t"
    "ds_read_b64_tr_b16 v[96:97], v248 offset:32768\n\t"
    "ds_read_b64_tr_b16 v[98:99], v248 offset:34816\n\t"
    "v_mfma_f32_16x16x32_bf16 v[56:59], v[132:135], v[204:207], v[56:59]\n\t"
    "ds_read_b64_tr_b16 v[100:101], v248 offset:36864\n\t"
    "v_mfma_f32_16x16x32_bf16 v[60:63], v[136:139], v[184:187], v[60:63]\n\t"
    "ds_read_b64_tr_b16 v[102:103], v248 offset:38912\n\t"
    "ds_read_b64_tr_b16 v[104:105], v249 offset:32768\n\t"
    "v_mfma_f32_16x16x32_bf16 v[60:63], v[140:143], v[196:199], v[60:63]\n\t"
    "ds_read_b64_tr_b16 v[106:107], v249 offset:34816\n\t"
    "v_mfma_f32_16x16x32_bf16 v[64:67], v[136:139], v[188:191], v[64:67]\n\t"
    "ds_read_b64_tr_b16 v[108:109], v249 offset:36864\n\t"
    "ds_read_b64_tr_b16 v[110:111], v249 offset:38912\n\t"
    "v_mfma_f32_16x16x32_bf16 v[64:67], v[140:143], v[200:203], v[64:67]\n\t"
    "ds_read_b64_tr_b16 v[112:113], v250 offset:32768\n\t"
    "v_mfma_f32_16x16x32_bf16 v[68:71], v[136:139], v[192:195], v[68:71]\n\t"
    "ds_read_b64_tr_b16 v[114:115], v250 offset:34816\n\t"
    "ds_read_b64_tr_b16 v[116:117], v250 offset:36864\n\t"
    "v_mfma_f32_16x16x32_bf16 v[68:71], v[140:143], v[204:207], v[68:71]\n\t"
    "ds_read_b64_tr_b16 v[118:119], v250 offset:38912\n\t"
    "v_mfma_f32_16x16x32_bf16 v[72:75], v[144:147], v[184:187], v[72:75]\n\t"
    "ds_read_b64_tr_b16 v[120:121], v251 offset:32768\n\t"
    "ds_read_b64_tr_b16 v[122:123], v251 offset:34816\n\t"
    "v_mfma_f32_16x16x32_bf16 v[72:75], v[148:151], v[196:199], v[72:75]\n\t"
    "ds_read_b64_tr_b16 v[124:125], v251 offset:36864\n\t"
    "v_mfma_f32_16x16x32_bf16 v[76:79], v[144:147], v[188:191], v[76:79]\n\t"
    "ds_read_b64_tr_b16 v[126:127], v251 offset:38912\n\t"
    "v_mfma_f32_16x16x32_bf16 v[76:79], v[148:151], v[200:203], v[76:79]\n\t"
    "v_mfma_f32_16x16x32_bf16 v[80:83], v[144:147], v[192:195], v[80:83]\n\t"
    "v_mfma_f32_16x16x32_bf16 v[80:83], v[148:151], v[204:207], v[80:83]\n\t"
    "v_mfma_f32_16x16x32_bf16 v[84:87], v[152:155], v[184:187], v[84:87]\n\t"
    "v_mfma_f32_16x16x32_bf16 v[84:87], v[156:159], v[196:199], v[84:87]\n\t"
    "v_mfma_f32_16x16x32_bf16 v[88:91], v[152:155], v[188:191], v[88:91]\n\t"
    "v_mfma_f32_16x16x32_bf16 v[88:91], v[156:159], v[200:203], v[88:91]\n\t"
    "v_mfma_f32_16x16x32_bf16 v[92:95], v[152:155], v[192:195], v[92:95]\n\t"
    "v_mfma_f32_16x16x32_bf16 v[92:95], v[156:159], v[204:207], v[92:95]\n\t"
    "s_sub_u32 s8, s8, 1\n\t"
    "s_cbranch_scc0 gqa_p1_0\n\t"
    "s_waitcnt lgkmcnt(0)\n\t"
    "s_barrier\n\t"
    "v_mfma_f32_16x16x32_bf16 v[0:3], v[96:99], v[160:163], v[0:3]\n\t"
    "ds_read_b64_tr_b16 v[128:129], v248 offset:49152\n\t"
    "ds_read_b64_tr_b16 v[130:131], v248 offset:51200\n\t"
    "v_mfma_f32_16x16x32_bf16 v[0:3], v[100:103], v[172:175], v[0:3]\n\t"
    "s_load_dwordx2 s[32:33], s[56:57], s13\n\t"
    "ds_read_b64_tr_b16 v[132:133], v248 offset:53248\n\t"
    "v_mfma_f32_16x16x32_bf16 v[4:7], v[96:99], v[164:167], v[4:7]\n\t"
    "ds_read_b64_tr_b16 v[134:135], v248 offset:55296\n\t"
    "v_mfma_f32_16x16x32_bf16 v[4:7], v[100:103], v[176:179], v[4:7]\n\t"
    "ds_read_b64_tr_b16 v[136:137], v249 offset:49152\n\t"
    "v_mfma_f32_16x16x32_bf16 v[8:11], v[96:99], v[168:171], v[8:11]\n\t"
    "ds_read_b64_tr_b16 v[138:139], v249 offset:51200\n\t"
    "v_mfma_f32_16x16x32_bf16 v[8:11], v[100:103], v[180:183], v[8:11]\n\t"
    "ds_read_b64_tr_b16 v[140:141], v249 offset:53248\n\t"
    "v_mfma_f32_16x16x32_bf16 v[12:15], v[104:107], v[160:163], v[12:15]\n\t"
    "ds_read_b64_tr_b16 v[142:143], v249 offset:55296\n\t"
    "v_mfma_f32_16x16x32_bf16 v[12:15], v[108:111], v[172:175], v[12:15]\n\t"
    "ds_read_b64_tr_b16 v[144:145], v250 offset:49152\n\t"
    "v_mfma_f32_16x16x32_bf16 v[16:19], v[104:107], v[164:167], v[16:19]\n\t"
    "ds_read_b64_tr_b16 v[146:147], v250 offset:51200\n\t"
    "v_mfma_f32_16x16x32_bf16 v[16:19], v[108:111], v[176:179], v[16:19]\n\t"
    "ds_read_b64_tr_b16 v[148:149], v250 offset:53248\n\t"
    "v_mfma_f32_16x16x32_bf16 v[20:23], v[104:107], v[168:171], v[20:23]\n\t"
    "ds_read_b64_tr_b16 v[150:151], v250 offset:55296\n\t"
    "v_mfma_f32_16x16x32_bf16 v[20:23], v[108:111], v[180:183], v[20:23]\n\t"
    "ds_read_b64_tr_b16 v[152:153], v251 offset:49152\n\t"
    "v_mfma_f32_16x16x32_bf16 v[24:27], v[112:115], v[160:163], v[24:27]\n\t"
    "ds_read_b64_tr_b16 v[154:155], v251 offset:51200\n\t"
    "v_mfma_f32_16x16x32_bf16 v[24:27], v[116:119], v[172:175], v[24:27]\n\t"
    "ds_read_b64_tr_b16 v[156:157], v251 offset:53248\n\t"
    "v_mfma_f32_16x16x32_bf16 v[28:31], v[112:115], v[164:167], v[28:31]\n\t"
    "ds_read_b64_tr_b16 v[158:159], v251 offset:55296\n\t"
    "v_mfma_f32_16x16x32_bf16 v[28:31], v[116:119], v[176:179], v[28:31]\n\t"
    "v_pk_mul_f32 v[0:1], v[0:1], s[28:29]\n\t"
    "v_pk_mul_f32 v[2:3], v[2:3], s[28:29]\n\t"
    "v_pk_mul_f32 v[12:13], v[12:13], s[28:29]\n\t"
    "v_pk_mul_f32 v[14:15], v[14:15], s[28:29]\n\t"
    "v_pk_mul_f32 v[4:5], v[4:5], s[28:29]\n\t"
    "v_pk_mul_f32 v[6:7], v[6:7], s[28:29]\n\t"
    "v_pk_mul_f32 v[16:17], v[16:17], s[28:29]\n\t"
    "v_pk_mul_f32 v[18:19], v[18:19], s[28:29]\n\t"
    "v_pk_mul_f32 v[8:9], v[8:9], s[28:29]\n\t"
    "v_pk_mul_f32 v[10:11], v[10:11], s[28:29]\n\t"
    "v_pk_mul_f32 v[20:21], v[20:21], s[28:29]\n\t"
    "v_pk_mul_f32 v[22:23], v[22:23], s[28:29]\n\t"
    "v_mfma_f32_16x16x32_bf16 v[32:35], v[112:115], v[168:171], v[32:35]\n\t"
    "v_xor_b32 v248, s37, v248\n\t"
    "v_xor_b32 v249, s37, v249\n\t"
    "v_mfma_f32_16x16x32_bf16 v[32:35], v[116:119], v[180:183], v[32:35]\n\t"
    "v_xor_b32 v250, s37, v250\n\t"
    "v_xor_b32 v251, s37, v251\n\t"
    "v_mfma_f32_16x16x32_bf16 v[36:39], v[120:123], v[160:163], v[36:39]\n\t"
    "v_mov_b32 v252, v254\n\t"
    "v_mov_b32 v253, v255\n\t"
    "v_mfma_f32_16x16x32_bf16 v[36:39], v[124:127], v[172:175], v[36:39]\n\t"
    "v_cvt_pk_bf16_f32 v184, v0, v1\n\t"
    "v_cvt_pk_bf16_f32 v185, v2, v3\n\t"
    "v_mfma_f32_16x16x32_bf16 v[40:43], v[120:123], v[164:167], v[40:43]\n\t"
    "v_cvt_pk_bf16_f32 v186, v12, v13\n\t"
    "v_cvt_pk_bf16_f32 v187, v14, v15\n\t"
    "v_mfma_f32_16x16x32_bf16 v[40:43], v[124:127], v[176:179], v[40:43]\n\t"
    "v_cvt_pk_bf16_f32 v200, v4, v5\n\t"
    "v_cvt_pk_bf16_f32 v201, v6, v7\n\t"
    "v_mfma_f32_16x16x32_bf16 v[44:47], v[120:123], v[168:171], v[44:47]\n\t"
    "v_cvt_pk_bf16_f32 v202, v16, v17\n\t"
    "v_cvt_pk_bf16_f32 v203, v18, v19\n\t"
    "v_mfma_f32_16x16x32_bf16 v[44:47], v[124:127], v[180:183], v[44:47]\n\t"
    "s_waitcnt lgkmcnt(0)\n\t"
    "v_cvt_pk_bf16_f32 v216, v8, v9\n\t"
    "v_mfma_f32_16x16x32_bf16 v[48:51], v[128:131], v[160:163], v[48:51]\n\t"
    "v_cvt_pk_bf16_f32 v217, v10, v11\n\t"
    "v_cvt_pk_bf16_f32 v218, v20, v21\n\t"
    "v_mfma_f32_16x16x32_bf16 v[48:51], v[132:135], v[172:175], v[48:51]\n\t"
    "v_cvt_pk_bf16_f32 v219, v22, v23\n\t"
    "s_xor_b32 s33, s33, 0x9e3779b9\n\t"
    "v_mfma_f32_16x16x32_bf16 v[52:55], v[128:131], v[164:167], v[52:55]\n\t"
    "s_barrier\n\t"
    "ds_read_b128 v[232:235], v252 offset:0\n\t"
    "v_mfma_f32_16x16x32_bf16 v[52:55], v[132:135], v[176:179], v[52:55]\n\t"
    "v_pk_mul_f32 v[24:25], v[24:25], s[28:29]\n\t"
    "v_pk_mul_f32 v[26:27], v[26:27], s[28:29]\n\t"
    "v_pk_mul_f32 v[36:37], v[36:37], s[28:29]\n\t"
    "v_pk_mul_f32 v[38:39], v[38:39], s[28:29]\n\t"
    "v_pk_mul_f32 v[28:29], v[28:29], s[28:29]\n\t"
    "v_pk_mul_f32 v[30:31], v[30:31], s[28:29]\n\t"
    "v_pk_mul_f32 v[40:41], v[40:41], s[28:29]\n\t"
    "v_pk_mul_f32 v[42:43], v[42:43], s[28:29]\n\t"
    "v_pk_mul_f32 v[32:33], v[32:33], s[28:29]\n\t"
    "v_pk_mul_f32 v[34:35], v[34:35], s[28:29]\n\t"
    "v_pk_mul_f32 v[44:45], v[44:45], s[28:29]\n\t"
    "v_pk_mul_f32 v[46:47], v[46:47], s[28:29]\n\t"
    "v_mfma_f32_16x16x32_bf16 v[56:59], v[128:131], v[168:171], v[56:59]\n\t"
    "v_mbcnt_lo_u32_b32 v254, -1, 0\n\t"
    "v_mbcnt_hi_u32_b32 v254, -1, v254\n\t"
    "v_mfma_f32_16x16x32_bf16 v[56:59], v[132:135], v[180:183], v[56:59]\n\t"
    "v_lshlrev_b32 v254, 4, v254\n\t"
    "v_add_u32 v255, s68, v254\n\t"
    "v_mfma_f32_16x16x32_bf16 v[60:63], v[136:139], v[160:163], v[60:63]\n\t"
    "v_add_u32 v254, s67, v254\n\t"
    "ds_read_b128 v[236:239], v252 offset:4096\n\t"
    "v_mfma_f32_16x16x32_bf16 v[60:63], v[140:143], v[172:175], v[60:63]\n\t"
    "ds_read_b128 v[240:243], v253 offset:0\n\t"
    "ds_read_b128 v[244:247], v253 offset:4096\n\t"
    "v_mfma_f32_16x16x32_bf16 v[64:67], v[136:139], v[164:167], v[64:67]\n\t"
    "v_permlane16_swap_b32 v184, v186\n\t"
    "v_mfma_f32_16x16x32_bf16 v[64:67], v[140:143], v[176:179], v[64:67]\n\t"
    "v_permlane16_swap_b32 v185, v187\n\t"
    "v_mfma_f32_16x16x32_bf16 v[68:71], v[136:139], v[168:171], v[68:71]\n\t"
    "v_permlane16_swap_b32 v200, v202\n\t"
    "v_mfma_f32_16x16x32_bf16 v[68:71], v[140:143], v[180:183], v[68:71]\n\t"
    "v_permlane16_swap_b32 v201, v203\n\t"
    "v_mfma_f32_16x16x32_bf16 v[72:75], v[144:147], v[160:163], v[72:75]\n\t"
    "v_permlane16_swap_b32 v216, v218\n\t"
    "v_mfma_f32_16x16x32_bf16 v[72:75], v[148:151], v[172:175], v[72:75]\n\t"
    "v_permlane16_swap_b32 v217, v219\n\t"
    "v_mfma_f32_16x16x32_bf16 v[76:79], v[144:147], v[164:167], v[76:79]\n\t"
    "v_cvt_pk_bf16_f32 v188, v24, v25\n\t"
    "v_cvt_pk_bf16_f32 v189, v26, v27\n\t"
    "v_mfma_f32_16x16x32_bf16 v[76:79], v[148:151], v[176:179], v[76:79]\n\t"
    "v_pk_mul_f32 v[48:49], v[48:49], s[28:29]\n\t"
    "v_pk_mul_f32 v[50:51], v[50:51], s[28:29]\n\t"
    "v_pk_mul_f32 v[60:61], v[60:61], s[28:29]\n\t"
    "v_pk_mul_f32 v[62:63], v[62:63], s[28:29]\n\t"
    "v_pk_mul_f32 v[52:53], v[52:53], s[28:29]\n\t"
    "v_pk_mul_f32 v[54:55], v[54:55], s[28:29]\n\t"
    "v_pk_mul_f32 v[64:65], v[64:65], s[28:29]\n\t"
    "v_pk_mul_f32 v[66:67], v[66:67], s[28:29]\n\t"
    "v_pk_mul_f32 v[56:57], v[56:57], s[28:29]\n\t"
    "v_pk_mul_f32 v[58:59], v[58:59], s[28:29]\n\t"
    "v_pk_mul_f32 v[68:69], v[68:69], s[28:29]\n\t"
    "v_pk_mul_f32 v[70:71], v[70:71], s[28:29]\n\t"
    "v_mfma_f32_16x16x32_bf16 v[80:83], v[144:147], v[168:171], v[80:83]\n\t"
    "v_cvt_pk_bf16_f32 v190, v36, v37\n\t"
    "v_cvt_pk_bf16_f32 v191, v38, v39\n\t"
    "v_mfma_f32_16x16x32_bf16 v[80:83], v[148:151], v[180:183], v[80:83]\n\t"
    "v_cvt_pk_bf16_f32 v204, v28, v29\n\t"
    "v_cvt_pk_bf16_f32 v205, v30, v31\n\t"
    "v_mfma_f32_16x16x32_bf16 v[84:87], v[152:155], v[160:163], v[84:87]\n\t"
    "v_cvt_pk_bf16_f32 v206, v40, v41\n\t"
    "v_cvt_pk_bf16_f32 v207, v42, v43\n\t"
    "v_mfma_f32_16x16x32_bf16 v[84:87], v[156:159], v[172:175], v[84:87]\n\t"
    "v_cvt_pk_bf16_f32 v220, v32, v33\n\t"
    "v_cvt_pk_bf16_f32 v221, v34, v35\n\t"
    "v_mfma_f32_16x16x32_bf16 v[88:91], v[152:155], v[164:167], v[88:91]\n\t"
    "v_cvt_pk_bf16_f32 v222, v44, v45\n\t"
    "v_cvt_pk_bf16_f32 v223, v46, v47\n\t"
    "v_mfma_f32_16x16x32_bf16 v[88:91], v[156:159], v[176:179], v[88:91]\n\t"
    "v_permlane16_swap_b32 v188, v190\n\t"
    "v_mfma_f32_16x16x32_bf16 v[92:95], v[152:155], v[168:171], v[92:95]\n\t"
    "v_permlane16_swap_b32 v189, v191\n\t"
    "v_mfma_f32_16x16x32_bf16 v[92:95], v[156:159], v[180:183], v[92:95]\n\t"
    "s_waitcnt lgkmcnt(2)\n\t"
    "ds_read_b128 v[176:179], v252 offset:2048\n\t"
    "v_mfma_f32_16x16x32_bf16 v[96:99], v[232:235], v[184:187], 0\n\t"
    "ds_read_b128 v[172:175], v252 offset:6144\n\t"
    "ds_read_b128 v[180:183], v253 offset:2048\n\t"
    "v_mfma_f32_16x16x32_bf16 v[100:103], v[232:235], v[200:203], 0\n\t"
    "v_permlane16_swap_b32 v204, v206\n\t"
    "v_mfma_f32_16x16x32_bf16 v[104:107], v[232:235], v[216:219], 0\n\t"
    "v_permlane16_swap_b32 v205, v207\n\t"
    "v_mfma_f32_16x16x32_bf16 v[108:111], v[236:239], v[184:187], 0\n\t"
    "v_pk_mul_f32 v[72:73], v[72:73], s[28:29]\n\t"
    "v_pk_mul_f32 v[74:75], v[74:75], s[28:29]\n\t"
    "v_pk_mul_f32 v[84:85], v[84:85], s[28:29]\n\t"
    "v_pk_mul_f32 v[86:87], v[86:87], s[28:29]\n\t"
    "v_pk_mul_f32 v[76:77], v[76:77], s[28:29]\n\t"
    "v_pk_mul_f32 v[78:79], v[78:79], s[28:29]\n\t"
    "v_pk_mul_f32 v[88:89], v[88:89], s[28:29]\n\t"
    "v_pk_mul_f32 v[90:91], v[90:91], s[28:29]\n\t"
    "v_pk_mul_f32 v[80:81], v[80:81], s[28:29]\n\t"
    "v_pk_mul_f32 v[82:83], v[82:83], s[28:29]\n\t"
    "v_pk_mul_f32 v[92:93], v[92:93], s[28:29]\n\t"
    "v_pk_mul_f32 v[94:95], v[94:95], s[28:29]\n\t"
    "v_mfma_f32_16x16x32_bf16 v[112:115], v[236:239], v[200:203], 0\n\t"
    "v_permlane16_swap_b32 v220, v222\n\t"
    "v_mfma_f32_16x16x32_bf16 v[116:119], v[236:239], v[216:219], 0\n\t"
    "v_permlane16_swap_b32 v221, v223\n\t"
    "s_waitcnt lgkmcnt(3)\n\t"
    "v_mfma_f32_16x16x32_bf16 v[96:99], v[240:243], v[188:191], v[96:99]\n\t"
    "ds_read_b128 v[236:239], v253 offset:6144\n\t"
    "v_cvt_pk_bf16_f32 v192, v48, v49\n\t"
    "v_mfma_f32_16x16x32_bf16 v[100:103], v[240:243], v[204:207], v[100:103]\n\t"
    "v_cvt_pk_bf16_f32 v193, v50, v51\n\t"
    "v_cvt_pk_bf16_f32 v194, v60, v61\n\t"
    "v_mfma_f32_16x16x32_bf16 v[104:107], v[240:243], v[220:223], v[104:107]\n\t"
    "v_cvt_pk_bf16_f32 v195, v62, v63\n\t"
    "v_cvt_pk_bf16_f32 v208, v52, v53\n\t"
    "v_mfma_f32_16x16x32_bf16 v[108:111], v[244:247], v[188:191], v[108:111]\n\t"
    "v_cvt_pk_bf16_f32 v209, v54, v55\n\t"
    "v_cvt_pk_bf16_f32 v210, v64, v65\n\t"
    "v_mfma_f32_16x16x32_bf16 v[112:115], v[244:247], v[204:207], v[112:115]\n\t"
    "v_cvt_pk_bf16_f32 v211, v66, v67\n\t"
    "v_cvt_pk_bf16_f32 v224, v56, v57\n\t"
    "v_mfma_f32_16x16x32_bf16 v[116:119], v[244:247], v[220:223], v[116:119]\n\t"
    "v_cvt_pk_bf16_f32 v225, v58, v59\n\t"
    "v_cvt_pk_bf16_f32 v226, v68, v69\n\t"
    "v_cvt_pk_bf16_f32 v227, v70, v71\n\t"
    "v_permlane16_swap_b32 v192, v194\n\t"
    "v_permlane16_swap_b32 v193, v195\n\t"
    "v_permlane16_swap_b32 v208, v210\n\t"
    "v_permlane16_swap_b32 v209, v211\n\t"
    "v_permlane16_swap_b32 v224, v226\n\t"
    "v_permlane16_swap_b32 v225, v227\n\t"
    "s_waitcnt lgkmcnt(2)\n\t"
    "v_mfma_f32_16x16x32_bf16 v[96:99], v[176:179], v[192:195], v[96:99]\n\t"
    "s_barrier\n\t"
    "ds_read_b128 v[232:235], v252 offset:16384\n\t"
    "v_mfma_f32_16x16x32_bf16 v[100:103], v[176:179], v[208:211], v[100:103]\n\t"
    "ds_read_b128 v[240:243], v253 offset:16384\n\t"
    "v_cvt_pk_bf16_f32 v196, v72, v73\n\t"
    "v_mfma_f32_16x16x32_bf16 v[104:107], v[176:179], v[224:227], v[104:107]\n\t"
    "s_add_u32 s34, s32, 1\n\t"
    "s_lshl_b32 s32, s34, 4\n\t"
    "v_mfma_f32_16x16x32_bf16 v[108:111], v[172:175], v[192:195], v[108:111]\n\t"
    "v_cvt_pk_bf16_f32 v197, v74, v75\n\t"
    "v_cvt_pk_bf16_f32 v198, v84, v85\n\t"
    "v_mfma_f32_16x16x32_bf16 v[112:115], v[172:175], v[208:211], v[112:115]\n\t"
    "v_cvt_pk_bf16_f32 v199, v86, v87\n\t"
    "v_cvt_pk_bf16_f32 v212, v76, v77\n\t"
    "v_mfma_f32_16x16x32_bf16 v[116:119], v[172:175], v[224:227], v[116:119]\n\t"
    "v_cvt_pk_bf16_f32 v213, v78, v79\n\t"
    "v_cvt_pk_bf16_f32 v214, v88, v89\n\t"
    "v_cvt_pk_bf16_f32 v215, v90, v91\n\t"
    "v_cvt_pk_bf16_f32 v228, v80, v81\n\t"
    "v_cvt_pk_bf16_f32 v229, v82, v83\n\t"
    "v_cvt_pk_bf16_f32 v230, v92, v93\n\t"
    "v_cvt_pk_bf16_f32 v231, v94, v95\n\t"
    "v_permlane16_swap_b32 v196, v198\n\t"
    "v_permlane16_swap_b32 v197, v199\n\t"
    "v_permlane16_swap_b32 v212, v214\n\t"
    "v_permlane16_swap_b32 v213, v215\n\t"
    "v_permlane16_swap_b32 v228, v230\n\t"
    "v_permlane16_swap_b32 v229, v231\n\t"
    "s_waitcnt lgkmcnt(2)\n\t"
    "v_mfma_f32_16x16x32_bf16 v[96:99], v[180:183], v[196:199], v[96:99]\n\t"
    "ds_read_b128 v[244:247], v252 offset:18432\n\t"
    "ds_read_b128 v[172:175], v253 offset:18432\n\t"
    "v_mfma_f32_16x16x32_bf16 v[100:103], v[180:183], v[212:215], v[100:103]\n\t"
    "v_mov_b32 v168, 0x3f803f80\n\t"
    "v_mov_b32 v169, 0x3f803f80\n\t"
    "v_mfma_f32_16x16x32_bf16 v[104:107], v[180:183], v[228:231], v[104:107]\n\t"
    "ds_read_b128 v[180:183], v252 offset:20480\n\t"
    "ds_read_b128 v[176:179], v253 offset:20480\n\t"
    "v_mfma_f32_16x16x32_bf16 v[108:111], v[236:239], v[196:199], v[108:111]\n\t"
    "v_mov_b32 v170, 0x3f803f80\n\t"
    "v_mov_b32 v171, 0x3f803f80\n\t"
    "v_mfma_f32_16x16x32_bf16 v[112:115], v[236:239], v[212:215], v[112:115]\n\t"
    "v_mfma_f32_16x16x32_bf16 v[116:119], v[236:239], v[228:231], v[116:119]\n\t"
    "v_max3_f32 v132, v96, v97, v98\n\t"
    "s_nop 2\n\t"
    "v_max3_f32 v135, v99, v108, v109\n\t"
    "v_max3_f32 v133, v100, v101, v102\n\t"
    "s_nop 0\n\t"
    "v_max3_f32 v136, v103, v112, v113\n\t"
    "v_max3_f32 v134, v104, v105, v106\n\t"
    "v_max3_f32 v137, v107, v116, v117\n\t"
    "v_max3_f32 v132, v132, v110, v111\n\t"
    "v_max3_f32 v133, v133, v114, v115\n\t"
    "v_max3_f32 v134, v134, v118, v119\n\t"
    "v_max_f32 v132, v132, v135\n\t"
    "v_max_f32 v133, v133, v136\n\t"
    "v_max_f32 v134, v134, v137\n\t"
    "v_mov_b32 v135, v132\n\t"
    "v_mov_b32 v136, v133\n\t"
    "v_mov_b32 v137, v134\n\t"
    "v_permlane32_swap_b32 v132, v135\n\t"
    "v_permlane32_swap_b32 v133, v136\n\t"
    "v_permlane32_swap_b32 v134, v137\n\t"
    "v_max_f32 v132, v132, v135\n\t"
    "v_max_f32 v133, v133, v136\n\t"
    "v_max_f32 v134, v134, v137\n\t"
    "v_mov_b32 v135, v132\n\t"
    "v_mov_b32 v136, v133\n\t"
    "v_mov_b32 v137, v134\n\t"
    "v_permlane16_swap_b32 v132, v135\n\t"
    "v_permlane16_swap_b32 v133, v136\n\t"
    "v_permlane16_swap_b32 v134, v137\n\t"
    "v_max_f32 v132, v132, v135\n\t"
    "v_max_f32 v133, v133, v136\n\t"
    "v_max_f32 v134, v134, v137\n\t"
    "v_xor_b32 v144, 0x80000000, v132\n\t"
    "v_xor_b32 v145, 0x80000000, v132\n\t"
    "v_xor_b32 v146, 0x80000000, v132\n\t"
    "v_xor_b32 v147, 0x80000000, v132\n\t"
    "v_xor_b32 v148, 0x80000000, v133\n\t"
    "v_xor_b32 v149, 0x80000000, v133\n\t"
    "v_xor_b32 v150, 0x80000000, v133\n\t"
    "v_xor_b32 v151, 0x80000000, v133\n\t"
    "v_xor_b32 v152, 0x80000000, v134\n\t"
    "v_xor_b32 v153, 0x80000000, v134\n\t"
    "v_xor_b32 v154, 0x80000000, v134\n\t"
    "v_xor_b32 v155, 0x80000000, v134\n\t"
    "s_waitcnt lgkmcnt(0)\n\t"
    "gqa_att:\n\t"
    "v_mfma_f32_16x16x32_bf16 v[84:87], v[232:235], v[184:187], v[144:147]\n\t"
    "ds_read_b128 v[236:239], v252 offset:22528\n\t"
    "v_add_f32 v96, v96, v144\n\t"
    "v_mfma_f32_16x16x32_bf16 v[84:87], v[240:243], v[188:191], v[84:87]\n\t"
    "v_exp_f32 v96, v96\n\t"
    "v_mfma_f32_16x16x32_bf16 v[88:91], v[232:235], v[200:203], v[148:151]\n\t"
    "v_add_f32 v97, v97, v144\n\t"
    "v_add_f32 v98, v98, v144\n\t"
    "v_mfma_f32_16x16x32_bf16 v[88:91], v[240:243], v[204:207], v[88:91]\n\t"
    "v_exp_f32 v97, v97\n\t"
    "v_mfma_f32_16x16x32_bf16 v[92:95], v[232:235], v[216:219], v[152:155]\n\t"
    "v_exp_f32 v98, v98\n\t"
    "v_mfma_f32_16x16x32_bf16 v[92:95], v[240:243], v[220:223], v[92:95]\n\t"
    "ds_read_b128 v[240:243], v253 offset:22528\n\t"
    "v_mfma_f32_16x16x32_bf16 v[84:87], v[244:247], v[192:195], v[84:87]\n\t"
    "ds_read_b64_tr_b16 v[232:233], v248 offset:8192\n\t"
    "ds_read_b64_tr_b16 v[234:235], v248 offset:10240\n\t"
    "v_mfma_f32_16x16x32_bf16 v[84:87], v[172:175], v[196:199], v[84:87]\n\t"
    "v_add_f32 v99, v99, v144\n\t"
    "v_add_f32 v108, v108, v144\n\t"
    "v_mfma_f32_16x16x32_bf16 v[88:91], v[244:247], v[208:211], v[88:91]\n\t"
    "v_exp_f32 v99, v99\n\t"
    "v_mfma_f32_16x16x32_bf16 v[88:91], v[172:175], v[212:215], v[88:91]\n\t"
    "v_exp_f32 v108, v108\n\t"
    "v_mfma_f32_16x16x32_bf16 v[92:95], v[244:247], v[224:227], v[92:95]\n\t"
    "ds_read_b64_tr_b16 v[244:245], v249 offset:8192\n\t"
    "ds_read_b64_tr_b16 v[246:247], v249 offset:10240\n\t"
    "v_mfma_f32_16x16x32_bf16 v[92:95], v[172:175], v[228:231], v[92:95]\n\t"
    "ds_read_b64_tr_b16 v[172:173], v250 offset:8192\n\t"
    "v_mfma_f32_16x16x32_bf16 v[72:75], v[180:183], v[184:187], v[144:147]\n\t"
    "ds_read_b64_tr_b16 v[174:175], v250 offset:10240\n\t"
    "v_add_f32 v109, v109, v144\n\t"
    "v_mfma_f32_16x16x32_bf16 v[72:75], v[176:179], v[188:191], v[72:75]\n\t"
    "v_exp_f32 v109, v109\n\t"
    "v_mfma_f32_16x16x32_bf16 v[76:79], v[180:183], v[200:203], v[148:151]\n\t"
    "v_add_f32 v110, v110, v144\n\t"
    "v_add_f32 v111, v111, v144\n\t"
    "v_mfma_f32_16x16x32_bf16 v[76:79], v[176:179], v[204:207], v[76:79]\n\t"
    "v_exp_f32 v110, v110\n\t"
    "v_mfma_f32_16x16x32_bf16 v[80:83], v[180:183], v[216:219], v[152:155]\n\t"
    "v_exp_f32 v111, v111\n\t"
    "v_mfma_f32_16x16x32_bf16 v[80:83], v[176:179], v[220:223], v[80:83]\n\t"
    "s_waitcnt lgkmcnt(6)\n\t"
    "ds_read_b64_tr_b16 v[176:177], v251 offset:8192\n\t"
    "v_mfma_f32_16x16x32_bf16 v[72:75], v[236:239], v[192:195], v[72:75]\n\t"
    "ds_read_b64_tr_b16 v[178:179], v251 offset:10240\n\t"
    "v_cvt_pk_bf16_f32 v120, v96, v97\n\t"
    "v_mfma_f32_16x16x32_bf16 v[72:75], v[240:243], v[196:199], v[72:75]\n\t"
    "v_cvt_pk_bf16_f32 v121, v98, v99\n\t"
    "v_add_f32 v100, v100, v148\n\t"
    "v_mfma_f32_16x16x32_bf16 v[76:79], v[236:239], v[208:211], v[76:79]\n\t"
    "v_exp_f32 v100, v100\n\t"
    "v_mfma_f32_16x16x32_bf16 v[76:79], v[240:243], v[212:215], v[76:79]\n\t"
    "v_add_f32 v101, v101, v148\n\t"
    "v_add_f32 v102, v102, v148\n\t"
    "v_mfma_f32_16x16x32_bf16 v[80:83], v[236:239], v[224:227], v[80:83]\n\t"
    "v_exp_f32 v101, v101\n\t"
    "v_mfma_f32_16x16x32_bf16 v[80:83], v[240:243], v[228:231], v[80:83]\n\t"
    "v_exp_f32 v102, v102\n\t"
    "v_add_f32 v103, v103, v148\n\t"
    "v_exp_f32 v103, v103\n\t"
    "v_cvt_pk_bf16_f32 v122, v108, v109\n\t"
    "v_cvt_pk_bf16_f32 v123, v110, v111\n\t"
    "v_add_f32 v112, v112, v148\n\t"
    "v_exp_f32 v112, v112\n\t"
    "v_add_f32 v113, v113, v148\n\t"
    "v_exp_f32 v113, v113\n\t"
    "v_add_f32 v114, v114, v148\n\t"
    "v_exp_f32 v114, v114\n\t"
    "v_add_f32 v115, v115, v148\n\t"
    "v_exp_f32 v115, v115\n\t"
    "v_mfma_f32_16x16x32_bf16 v[156:159], v[168:171], v[120:123], 0\n\t"
    "v_cvt_pk_bf16_f32 v124, v100, v101\n\t"
    "v_cvt_pk_bf16_f32 v125, v102, v103\n\t"
    "v_add_f32 v104, v104, v152\n\t"
    "v_exp_f32 v104, v104\n\t"
    "v_add_f32 v105, v105, v152\n\t"
    "v_exp_f32 v105, v105\n\t"
    "v_add_f32 v106, v106, v152\n\t"
    "v_exp_f32 v106, v106\n\t"
    "v_add_f32 v107, v107, v152\n\t"
    "v_exp_f32 v107, v107\n\t"
    "v_cvt_pk_bf16_f32 v126, v112, v113\n\t"
    "v_cvt_pk_bf16_f32 v127, v114, v115\n\t"
    "v_add_f32 v116, v116, v152\n\t"
    "v_exp_f32 v116, v116\n\t"
    "v_add_f32 v117, v117, v152\n\t"
    "v_exp_f32 v117, v117\n\t"
    "v_add_f32 v118, v118, v152\n\t"
    "v_exp_f32 v118, v118\n\t"
    "v_add_f32 v119, v119, v152\n\t"
    "v_exp_f32 v119, v119\n\t"
    "v_mfma_f32_16x16x32_bf16 v[160:163], v[168:171], v[124:127], 0\n\t"
    "v_cvt_pk_bf16_f32 v128, v104, v105\n\t"
    "v_cvt_pk_bf16_f32 v129, v106, v107\n\t"
    "v_cvt_pk_bf16_f32 v130, v116, v117\n\t"
    "v_cvt_pk_bf16_f32 v131, v118, v119\n\t"
    "s_nop 1\n\t"
    "v_mfma_f32_16x16x32_bf16 v[164:167], v[168:171], v[128:131], 0\n\t"
    "s_waitcnt lgkmcnt(4)\n\t"
    "ds_read_b64_tr_b16 v[236:237], v248 offset:12288\n\t"
    "v_mfma_f32_16x16x32_bf16 v[0:3], v[232:235], v[120:123], 0\n\t"
    "ds_read_b64_tr_b16 v[238:239], v248 offset:14336\n\t"
    "ds_read_b64_tr_b16 v[240:241], v249 offset:12288\n\t"
    "v_mfma_f32_16x16x32_bf16 v[4:7], v[232:235], v[124:127], 0\n\t"
    "ds_read_b64_tr_b16 v[242:243], v249 offset:14336\n\t"
    "ds_read_b64_tr_b16 v[180:181], v250 offset:12288\n\t"
    "v_mfma_f32_16x16x32_bf16 v[8:11], v[232:235], v[128:131], 0\n\t"
    "v_exp_f32 v72, v72\n\t"
    "v_mfma_f32_16x16x32_bf16 v[12:15], v[244:247], v[120:123], 0\n\t"
    "v_exp_f32 v73, v73\n\t"
    "v_mfma_f32_16x16x32_bf16 v[16:19], v[244:247], v[124:127], 0\n\t"
    "ds_read_b64_tr_b16 v[182:183], v250 offset:14336\n\t"
    "v_cvt_pk_bf16_f32 v134, v72, v73\n\t"
    "v_mfma_f32_16x16x32_bf16 v[20:23], v[244:247], v[128:131], 0\n\t"
    "s_waitcnt lgkmcnt(6)\n\t"
    "ds_read_b64_tr_b16 v[244:245], v251 offset:12288\n\t"
    "v_mfma_f32_16x16x32_bf16 v[24:27], v[172:175], v[120:123], 0\n\t"
    "v_exp_f32 v74, v74\n\t"
    "v_mfma_f32_16x16x32_bf16 v[28:31], v[172:175], v[124:127], 0\n\t"
    "v_exp_f32 v75, v75\n\t"
    "v_mfma_f32_16x16x32_bf16 v[32:35], v[172:175], v[128:131], 0\n\t"
    "ds_read_b64_tr_b16 v[246:247], v251 offset:14336\n\t"
    "s_barrier\n\t"
    "v_mfma_f32_16x16x32_bf16 v[36:39], v[176:179], v[120:123], 0\n\t"
    "s_add_u32 s13, s12, 0x8000\n\t"
    "s_and_b32 s13, s13, 0x1ffff\n\t"
    "v_mfma_f32_16x16x32_bf16 v[40:43], v[176:179], v[124:127], 0\n\t"
    "s_sub_u32 s11, s13, s12\n\t"
    "s_mov_b32 s12, s13\n\t"
    "v_mfma_f32_16x16x32_bf16 v[44:47], v[176:179], v[128:131], 0\n\t"
    "s_waitcnt lgkmcnt(4)\n\t"
    "v_add_u32 v252, s11, v252\n\t"
    "v_mfma_f32_16x16x32_bf16 v[48:51], v[236:239], v[120:123], 0\n\t"
    "v_add_u32 v253, s11, v253\n\t"
    "ds_read_b128 v[172:175], v252 offset:0\n\t"
    "v_mfma_f32_16x16x32_bf16 v[52:55], v[236:239], v[124:127], 0\n\t"
    "ds_read_b128 v[232:235], v253 offset:0\n\t"
    "v_cvt_pk_bf16_f32 v135, v74, v75\n\t"
    "v_mfma_f32_16x16x32_bf16 v[56:59], v[236:239], v[128:131], 0\n\t"
    "v_exp_f32 v76, v76\n\t"
    "v_mfma_f32_16x16x32_bf16 v[60:63], v[240:243], v[120:123], 0\n\t"
    "v_exp_f32 v77, v77\n\t"
    "v_mfma_f32_16x16x32_bf16 v[64:67], v[240:243], v[124:127], 0\n\t"
    "v_exp_f32 v78, v78\n\t"
    "v_exp_f32 v79, v79\n\t"
    "v_exp_f32 v80, v80\n\t"
    "v_exp_f32 v81, v81\n\t"
    "v_exp_f32 v82, v82\n\t"
    "v_exp_f32 v83, v83\n\t"
    "v_mfma_f32_16x16x32_bf16 v[68:71], v[240:243], v[128:131], 0\n\t"
    "v_cvt_pk_bf16_f32 v138, v76, v77\n\t"
    "v_cvt_pk_bf16_f32 v139, v78, v79\n\t"
    "v_cvt_pk_bf16_f32 v142, v80, v81\n\t"
    "v_cvt_pk_bf16_f32 v143, v82, v83\n\t"
    "s_waitcnt lgkmcnt(2)\n\t"
    "v_mfma_f32_16x16x32_bf16 v[72:75], v[180:183], v[120:123], 0\n\t"
    "ds_read_b128 v[236:239], v252 offset:2048\n\t"
    "ds_read_b128 v[240:243], v253 offset:2048\n\t"
    "v_mfma_f32_16x16x32_bf16 v[76:79], v[180:183], v[124:127], 0\n\t"
    "v_exp_f32 v84, v84\n\t"
    "v_exp_f32 v85, v85\n\t"
    "v_exp_f32 v86, v86\n\t"
    "v_exp_f32 v87, v87\n\t"
    "v_exp_f32 v88, v88\n\t"
    "v_exp_f32 v89, v89\n\t"
    "v_exp_f32 v90, v90\n\t"
    "v_exp_f32 v91, v91\n\t"
    "v_exp_f32 v92, v92\n\t"
    "v_exp_f32 v93, v93\n\t"
    "v_exp_f32 v94, v94\n\t"
    "v_exp_f32 v95, v95\n\t"
    "v_mfma_f32_16x16x32_bf16 v[80:83], v[180:183], v[128:131], 0\n\t"
    "v_cvt_pk_bf16_f32 v132, v84, v85\n\t"
    "v_cvt_pk_bf16_f32 v133, v86, v87\n\t"
    "v_cvt_pk_bf16_f32 v136, v88, v89\n\t"
    "v_cvt_pk_bf16_f32 v137, v90, v91\n\t"
    "v_cvt_pk_bf16_f32 v140, v92, v93\n\t"
    "v_cvt_pk_bf16_f32 v141, v94, v95\n\t"
    "v_mfma_f32_16x16x32_bf16 v[84:87], v[244:247], v[120:123], 0\n\t"
    "ds_read_b128 v[176:179], v252 offset:4096\n\t"
    "v_mfma_f32_16x16x32_bf16 v[88:91], v[244:247], v[124:127], 0\n\t"
    "v_mfma_f32_16x16x32_bf16 v[92:95], v[244:247], v[128:131], 0\n\t"
    "s_waitcnt lgkmcnt(3)\n\t"
    "ds_read_b128 v[244:247], v253 offset:4096\n\t"
    "v_mfma_f32_16x16x32_bf16 v[96:99], v[172:175], v[184:187], v[144:147]\n\t"
    "ds_read_b128 v[180:183], v252 offset:6144\n\t"
    "v_mfma_f32_16x16x32_bf16 v[96:99], v[232:235], v[188:191], v[96:99]\n\t"
    "v_mfma_f32_16x16x32_bf16 v[100:103], v[172:175], v[200:203], v[148:151]\n\t"
    "v_mfma_f32_16x16x32_bf16 v[100:103], v[232:235], v[204:207], v[100:103]\n\t"
    "v_mfma_f32_16x16x32_bf16 v[104:107], v[172:175], v[216:219], v[152:155]\n\t"
    "ds_read_b128 v[172:175], v253 offset:6144\n\t"
    "v_mfma_f32_16x16x32_bf16 v[104:107], v[232:235], v[220:223], v[104:107]\n\t"
    "s_waitcnt lgkmcnt(4)\n\t"
    "ds_read_b64_tr_b16 v[232:233], v248 offset:24576\n\t"
    "v_mfma_f32_16x16x32_bf16 v[96:99], v[236:239], v[192:195], v[96:99]\n\t"
    "ds_read_b64_tr_b16 v[234:235], v248 offset:26624\n\t"
    "v_mfma_f32_16x16x32_bf16 v[96:99], v[240:243], v[196:199], v[96:99]\n\t"
    "v_mfma_f32_16x16x32_bf16 v[100:103], v[236:239], v[208:211], v[100:103]\n\t"
    "v_mfma_f32_16x16x32_bf16 v[100:103], v[240:243], v[212:215], v[100:103]\n\t"
    "v_mfma_f32_16x16x32_bf16 v[104:107], v[236:239], v[224:227], v[104:107]\n\t"
    "ds_read_b64_tr_b16 v[236:237], v249 offset:24576\n\t"
    "ds_read_b64_tr_b16 v[238:239], v249 offset:26624\n\t"
    "v_mfma_f32_16x16x32_bf16 v[104:107], v[240:243], v[228:231], v[104:107]\n\t"
    "s_waitcnt lgkmcnt(6)\n\t"
    "ds_read_b64_tr_b16 v[240:241], v250 offset:24576\n\t"
    "v_mfma_f32_16x16x32_bf16 v[108:111], v[176:179], v[184:187], v[144:147]\n\t"
    "v_exp_f32 v96, v96\n\t"
    "v_mfma_f32_16x16x32_bf16 v[108:111], v[244:247], v[188:191], v[108:111]\n\t"
    "v_exp_f32 v97, v97\n\t"
    "v_mfma_f32_16x16x32_bf16 v[112:115], v[176:179], v[200:203], v[148:151]\n\t"
    "ds_read_b64_tr_b16 v[242:243], v250 offset:26624\n\t"
    "v_cvt_pk_bf16_f32 v120, v96, v97\n\t"
    "v_mfma_f32_16x16x32_bf16 v[112:115], v[244:247], v[204:207], v[112:115]\n\t"
    "v_exp_f32 v98, v98\n\t"
    "v_mfma_f32_16x16x32_bf16 v[116:119], v[176:179], v[216:219], v[152:155]\n\t"
    "v_exp_f32 v99, v99\n\t"
    "v_mfma_f32_16x16x32_bf16 v[116:119], v[244:247], v[220:223], v[116:119]\n\t"
    "s_waitcnt lgkmcnt(6)\n\t"
    "ds_read_b64_tr_b16 v[244:245], v251 offset:24576\n\t"
    "v_mfma_f32_16x16x32_bf16 v[108:111], v[180:183], v[192:195], v[108:111]\n\t"
    "ds_read_b64_tr_b16 v[246:247], v251 offset:26624\n\t"
    "ds_read_b64_tr_b16 v[176:177], v248 offset:28672\n\t"
    "v_mfma_f32_16x16x32_bf16 v[108:111], v[172:175], v[196:199], v[108:111]\n\t"
    "ds_read_b64_tr_b16 v[178:179], v248 offset:30720\n\t"
    "v_cvt_pk_bf16_f32 v121, v98, v99\n\t"
    "v_mfma_f32_16x16x32_bf16 v[112:115], v[180:183], v[208:211], v[112:115]\n\t"
    "v_exp_f32 v100, v100\n\t"
    "v_mfma_f32_16x16x32_bf16 v[112:115], v[172:175], v[212:215], v[112:115]\n\t"
    "v_exp_f32 v101, v101\n\t"
    "v_mfma_f32_16x16x32_bf16 v[116:119], v[180:183], v[224:227], v[116:119]\n\t"
    "v_exp_f32 v102, v102\n\t"
    "v_mfma_f32_16x16x32_bf16 v[116:119], v[172:175], v[228:231], v[116:119]\n\t"
    "ds_read_b64_tr_b16 v[172:173], v249 offset:28672\n\t"
    "ds_read_b64_tr_b16 v[174:175], v249 offset:30720\n\t"
    "v_mfma_f32_16x16x32_bf16 v[156:159], v[168:171], v[132:135], v[156:159]\n\t"
    "ds_read_b64_tr_b16 v[180:181], v250 offset:28672\n\t"
    "ds_read_b64_tr_b16 v[182:183], v250 offset:30720\n\t"
    "v_mfma_f32_16x16x32_bf16 v[160:163], v[168:171], v[136:139], v[160:163]\n\t"
    "v_exp_f32 v103, v103\n\t"
    "v_mfma_f32_16x16x32_bf16 v[164:167], v[168:171], v[140:143], v[164:167]\n\t"
    "s_waitcnt lgkmcnt(10)\n\t"
    "v_cvt_pk_bf16_f32 v124, v100, v101\n\t"
    "v_mfma_f32_16x16x32_bf16 v[0:3], v[232:235], v[132:135], v[0:3]\n\t"
    "v_exp_f32 v104, v104\n\t"
    "v_mfma_f32_16x16x32_bf16 v[4:7], v[232:235], v[136:139], v[4:7]\n\t"
    "v_exp_f32 v105, v105\n\t"
    "v_mfma_f32_16x16x32_bf16 v[8:11], v[232:235], v[140:143], v[8:11]\n\t"
    "v_cvt_pk_bf16_f32 v125, v102, v103\n\t"
    "v_cvt_pk_bf16_f32 v128, v104, v105\n\t"
    "v_mfma_f32_16x16x32_bf16 v[12:15], v[236:239], v[132:135], v[12:15]\n\t"
    "v_exp_f32 v106, v106\n\t"
    "v_mfma_f32_16x16x32_bf16 v[16:19], v[236:239], v[136:139], v[16:19]\n\t"
    "v_exp_f32 v107, v107\n\t"
    "v_mfma_f32_16x16x32_bf16 v[20:23], v[236:239], v[140:143], v[20:23]\n\t"
    "s_waitcnt lgkmcnt(6)\n\t"
    "ds_read_b64_tr_b16 v[236:237], v251 offset:28672\n\t"
    "v_mfma_f32_16x16x32_bf16 v[24:27], v[240:243], v[132:135], v[24:27]\n\t"
    "ds_read_b64_tr_b16 v[238:239], v251 offset:30720\n\t"
    "s_barrier\n\t"
    "v_mfma_f32_16x16x32_bf16 v[28:31], v[240:243], v[136:139], v[28:31]\n\t"
    "ds_read_b128 v[232:235], v252 offset:16384\n\t"
    "v_cvt_pk_bf16_f32 v129, v106, v107\n\t"
    "v_mfma_f32_16x16x32_bf16 v[32:35], v[240:243], v[140:143], v[32:35]\n\t"
    "v_exp_f32 v108, v108\n\t"
    "v_mfma_f32_16x16x32_bf16 v[36:39], v[244:247], v[132:135], v[36:39]\n\t"
    "v_exp_f32 v109, v109\n\t"
    "v_mfma_f32_16x16x32_bf16 v[40:43], v[244:247], v[136:139], v[40:43]\n\t"
    "ds_read_b128 v[240:243], v253 offset:16384\n\t"
    "v_cvt_pk_bf16_f32 v122, v108, v109\n\t"
    "v_mfma_f32_16x16x32_bf16 v[44:47], v[244:247], v[140:143], v[44:47]\n\t"
    "s_waitcnt lgkmcnt(6)\n\t"
    "ds_read_b128 v[244:247], v252 offset:18432\n\t"
    "v_mfma_f32_16x16x32_bf16 v[48:51], v[176:179], v[132:135], v[48:51]\n\t"
    "v_exp_f32 v110, v110\n\t"
    "v_mfma_f32_16x16x32_bf16 v[52:55], v[176:179], v[136:139], v[52:55]\n\t"
    "v_exp_f32 v111, v111\n\t"
    "v_mfma_f32_16x16x32_bf16 v[56:59], v[176:179], v[140:143], v[56:59]\n\t"
    "v_exp_f32 v112, v112\n\t"
    "v_mfma_f32_16x16x32_bf16 v[60:63], v[172:175], v[132:135], v[60:63]\n\t"
    "v_exp_f32 v113, v113\n\t"
    "v_mfma_f32_16x16x32_bf16 v[64:67], v[172:175], v[136:139], v[64:67]\n\t"
    "v_cvt_pk_bf16_f32 v123, v110, v111\n\t"
    "v_cvt_pk_bf16_f32 v126, v112, v113\n\t"
    "v_mfma_f32_16x16x32_bf16 v[68:71], v[172:175], v[140:143], v[68:71]\n\t"
    "s_waitcnt lgkmcnt(3)\n\t"
    "ds_read_b128 v[172:175], v253 offset:18432\n\t"
    "v_mfma_f32_16x16x32_bf16 v[72:75], v[180:183], v[132:135], v[72:75]\n\t"
    "v_exp_f32 v114, v114\n\t"
    "v_mfma_f32_16x16x32_bf16 v[76:79], v[180:183], v[136:139], v[76:79]\n\t"
    "v_exp_f32 v115, v115\n\t"
    "v_mfma_f32_16x16x32_bf16 v[80:83], v[180:183], v[140:143], v[80:83]\n\t"
    "ds_read_b128 v[180:183], v252 offset:20480\n\t"
    "ds_read_b128 v[176:179], v253 offset:20480\n\t"
    "v_mfma_f32_16x16x32_bf16 v[84:87], v[236:239], v[132:135], v[84:87]\n\t"
    "v_exp_f32 v116, v116\n\t"
    "v_mfma_f32_16x16x32_bf16 v[88:91], v[236:239], v[136:139], v[88:91]\n\t"
    "v_exp_f32 v117, v117\n\t"
    "v_mfma_f32_16x16x32_bf16 v[92:95], v[236:239], v[140:143], v[92:95]\n\t"
    "s_mov_b32 s8, 5\n\t"
    "gqa_at_0:\n\t"
    "s_waitcnt lgkmcnt(4)\n\t"
    "ds_read_b128 v[236:239], v252 offset:22528\n\t"
    "v_mfma_f32_16x16x32_bf16 v[96:99], v[232:235], v[184:187], v[144:147]\n\t"
    "v_cvt_pk_bf16_f32 v127, v114, v115\n\t"
    "v_cvt_pk_bf16_f32 v130, v116, v117\n\t"
    "v_mfma_f32_16x16x32_bf16 v[96:99], v[240:243], v[188:191], v[96:99]\n\t"
    "v_exp_f32 v118, v118\n\t"
    "v_mfma_f32_16x16x32_bf16 v[100:103], v[232:235], v[200:203], v[148:151]\n\t"
    "v_exp_f32 v119, v119\n\t"
    "v_mfma_f32_16x16x32_bf16 v[100:103], v[240:243], v[204:207], v[100:103]\n\t"
    "v_cvt_pk_bf16_f32 v131, v118, v119\n\t"
    "v_mfma_f32_16x16x32_bf16 v[104:107], v[232:235], v[216:219], v[152:155]\n\t"
    "v_mfma_f32_16x16x32_bf16 v[104:107], v[240:243], v[220:223], v[104:107]\n\t"
    "s_waitcnt lgkmcnt(3)\n\t"
    "ds_read_b128 v[240:243], v253 offset:22528\n\t"
    "v_mfma_f32_16x16x32_bf16 v[96:99], v[244:247], v[192:195], v[96:99]\n\t"
    "v_add_u32 v248, s11, v248\n\t"
    "v_add_u32 v249, s11, v249\n\t"
    "v_mfma_f32_16x16x32_bf16 v[96:99], v[172:175], v[196:199], v[96:99]\n\t"
    "v_add_u32 v250, s11, v250\n\t"
    "v_add_u32 v251, s11, v251\n\t"
    "v_mfma_f32_16x16x32_bf16 v[100:103], v[244:247], v[208:211], v[100:103]\n\t"
    "ds_read_b64_tr_b16 v[232:233], v248 offset:8192\n\t"
    "ds_read_b64_tr_b16 v[234:235], v248 offset:10240\n\t"
    "v_mfma_f32_16x16x32_bf16 v[100:103], v[172:175], v[212:215], v[100:103]\n\t"
    "v_mfma_f32_16x16x32_bf16 v[104:107], v[244:247], v[224:227], v[104:107]\n\t"
    "ds_read_b64_tr_b16 v[244:245], v249 offset:8192\n\t"
    "ds_read_b64_tr_b16 v[246:247], v249 offset:10240\n\t"
    "v_mfma_f32_16x16x32_bf16 v[104:107], v[172:175], v[228:231], v[104:107]\n\t"
    "s_waitcnt lgkmcnt(6)\n\t"
    "ds_read_b64_tr_b16 v[172:173], v250 offset:8192\n\t"
    "v_mfma_f32_16x16x32_bf16 v[108:111], v[180:183], v[184:187], v[144:147]\n\t"
    "v_exp_f32 v96, v96\n\t"
    "v_mfma_f32_16x16x32_bf16 v[108:111], v[176:179], v[188:191], v[108:111]\n\t"
    "v_exp_f32 v97, v97\n\t"
    "v_mfma_f32_16x16x32_bf16 v[112:115], v[180:183], v[200:203], v[148:151]\n\t"
    "ds_read_b64_tr_b16 v[174:175], v250 offset:10240\n\t"
    "v_cvt_pk_bf16_f32 v132, v96, v97\n\t"
    "v_mfma_f32_16x16x32_bf16 v[112:115], v[176:179], v[204:207], v[112:115]\n\t"
    "v_exp_f32 v98, v98\n\t"
    "v_mfma_f32_16x16x32_bf16 v[116:119], v[180:183], v[216:219], v[152:155]\n\t"
    "v_exp_f32 v99, v99\n\t"
    "v_mfma_f32_16x16x32_bf16 v[116:119], v[176:179], v[220:223], v[116:119]\n\t"
    "s_waitcnt lgkmcnt(6)\n\t"
    "ds_read_b64_tr_b16 v[176:177], v251 offset:8192\n\t"
    "v_mfma_f32_16x16x32_bf16 v[108:111], v[236:239], v[192:195], v[108:111]\n\t"
    "ds_read_b64_tr_b16 v[178:179], v251 offset:10240\n\t"
    "v_cvt_pk_bf16_f32 v133, v98, v99\n\t"
    "v_mfma_f32_16x16x32_bf16 v[108:111], v[240:243], v[196:199], v[108:111]\n\t"
    "v_exp_f32 v100, v100\n\t"
    "v_mfma_f32_16x16x32_bf16 v[112:115], v[236:239], v[208:211], v[112:115]\n\t"
    "v_exp_f32 v101, v101\n\t"
    "v_mfma_f32_16x16x32_bf16 v[112:115], v[240:243], v[212:215], v[112:115]\n\t"
    "v_exp_f32 v102, v102\n\t"
    "v_mfma_f32_16x16x32_bf16 v[116:119], v[236:239], v[224:227], v[116:119]\n\t"
    "ds_read_b64_tr_b16 v[236:237], v248 offset:12288\n\t"
    "ds_read_b64_tr_b16 v[238:239], v248 offset:14336\n\t"
    "v_mfma_f32_16x16x32_bf16 v[116:119], v[240:243], v[228:231], v[116:119]\n\t"
    "ds_read_b64_tr_b16 v[240:241], v249 offset:12288\n\t"
    "ds_read_b64_tr_b16 v[242:243], v249 offset:14336\n\t"
    "v_mfma_f32_16x16x32_bf16 v[156:159], v[168:171], v[120:123], v[156:159]\n\t"
    "ds_read_b64_tr_b16 v[180:181], v250 offset:12288\n\t"
    "ds_read_b64_tr_b16 v[182:183], v250 offset:14336\n\t"
    "v_mfma_f32_16x16x32_bf16 v[160:163], v[168:171], v[124:127], v[160:163]\n\t"
    "v_exp_f32 v103, v103\n\t"
    "v_mfma_f32_16x16x32_bf16 v[164:167], v[168:171], v[128:131], v[164:167]\n\t"
    "s_waitcnt lgkmcnt(10)\n\t"
    "v_cvt_pk_bf16_f32 v136, v100, v101\n\t"
    "v_mfma_f32_16x16x32_bf16 v[0:3], v[232:235], v[120:123], v[0:3]\n\t"
    "v_exp_f32 v104, v104\n\t"
    "v_mfma_f32_16x16x32_bf16 v[4:7], v[232:235], v[124:127], v[4:7]\n\t"
    "v_exp_f32 v105, v105\n\t"
    "v_mfma_f32_16x16x32_bf16 v[8:11], v[232:235], v[128:131], v[8:11]\n\t"
    "v_cvt_pk_bf16_f32 v137, v102, v103\n\t"
    "v_cvt_pk_bf16_f32 v140, v104, v105\n\t"
    "v_mfma_f32_16x16x32_bf16 v[12:15], v[244:247], v[120:123], v[12:15]\n\t"
    "v_exp_f32 v106, v106\n\t"
    "v_mfma_f32_16x16x32_bf16 v[16:19], v[244:247], v[124:127], v[16:19]\n\t"
    "v_exp_f32 v107, v107\n\t"
    "v_mfma_f32_16x16x32_bf16 v[20:23], v[244:247], v[128:131], v[20:23]\n\t"
    "s_waitcnt lgkmcnt(6)\n\t"
    "ds_read_b64_tr_b16 v[244:245], v251 offset:12288\n\t"
    "v_mfma_f32_16x16x32_bf16 v[24:27], v[172:175], v[120:123], v[24:27]\n\t"
    "ds_read_b64_tr_b16 v[246:247], v251 offset:14336\n\t"
    "v_cvt_pk_bf16_f32 v141, v106, v107\n\t"
    "v_mfma_f32_16x16x32_bf16 v[28:31], v[172:175], v[124:127], v[28:31]\n\t"
    "v_exp_f32 v108, v108\n\t"
    "v_mfma_f32_16x16x32_bf16 v[32:35], v[172:175], v[128:131], v[32:35]\n\t"
    "s_barrier\n\t"
    "v_mfma_f32_16x16x32_bf16 v[36:39], v[176:179], v[120:123], v[36:39]\n\t"
    "s_add_u32 s13, s12, 0x8000\n\t"
    "s_and_b32 s13, s13, 0x1ffff\n\t"
    "v_mfma_f32_16x16x32_bf16 v[40:43], v[176:179], v[124:127], v[40:43]\n\t"
    "s_sub_u32 s11, s13, s12\n\t"
    "s_mov_b32 s12, s13\n\t"
    "v_mfma_f32_16x16x32_bf16 v[44:47], v[176:179], v[128:131], v[44:47]\n\t"
    "s_waitcnt lgkmcnt(4)\n\t"
    "v_add_u32 v252, s11, v252\n\t"
    "v_mfma_f32_16x16x32_bf16 v[48:51], v[236:239], v[120:123], v[48:51]\n\t"
    "v_add_u32 v253, s11, v253\n\t"
    "ds_read_b128 v[172:175], v252 offset:0\n\t"
    "v_mfma_f32_16x16x32_bf16 v[52:55], v[236:239], v[124:127], v[52:55]\n\t"
    "v_exp_f32 v109, v109\n\t"
    "v_mfma_f32_16x16x32_bf16 v[56:59], v[236:239], v[128:131], v[56:59]\n\t"
    "ds_read_b128 v[232:235], v253 offset:0\n\t"
    "ds_read_b128 v[236:239], v252 offset:2048\n\t"
    "v_mfma_f32_16x16x32_bf16 v[60:63], v[240:243], v[120:123], v[60:63]\n\t"
    "v_exp_f32 v110, v110\n\t"
    "v_mfma_f32_16x16x32_bf16 v[64:67], v[240:243], v[124:127], v[64:67]\n\t"
    "v_exp_f32 v111, v111\n\t"
    "v_mfma_f32_16x16x32_bf16 v[68:71], v[240:243], v[128:131], v[68:71]\n\t"
    "s_waitcnt lgkmcnt(3)\n\t"
    "ds_read_b128 v[240:243], v253 offset:2048\n\t"
    "v_mfma_f32_16x16x32_bf16 v[72:75], v[180:183], v[120:123], v[72:75]\n\t"
    "ds_read_b128 v[176:179], v252 offset:4096\n\t"
    "v_cvt_pk_bf16_f32 v134, v108, v109\n\t"
    "v_mfma_f32_16x16x32_bf16 v[76:79], v[180:183], v[124:127], v[76:79]\n\t"
    "v_exp_f32 v112, v112\n\t"
    "v_mfma_f32_16x16x32_bf16 v[80:83], v[180:183], v[128:131], v[80:83]\n\t"
    "v_exp_f32 v113, v113\n\t"
    "v_mfma_f32_16x16x32_bf16 v[84:87], v[244:247], v[120:123], v[84:87]\n\t"
    "v_cvt_pk_bf16_f32 v135, v110, v111\n\t"
    "v_cvt_pk_bf16_f32 v138, v112, v113\n\t"
    "v_mfma_f32_16x16x32_bf16 v[88:91], v[244:247], v[124:127], v[88:91]\n\t"
    "v_exp_f32 v114, v114\n\t"
    "v_mfma_f32_16x16x32_bf16 v[92:95], v[244:247], v[128:131], v[92:95]\n\t"
    "s_waitcnt lgkmcnt(3)\n\t"
    "ds_read_b128 v[244:247], v253 offset:4096\n\t"
    "v_mfma_f32_16x16x32_bf16 v[96:99], v[172:175], v[184:187], v[144:147]\n\t"
    "v_exp_f32 v115, v115\n\t"
    "v_mfma_f32_16x16x32_bf16 v[96:99], v[232:235], v[188:191], v[96:99]\n\t"
    "ds_read_b128 v[180:183], v252 offset:6144\n\t"
    "v_cvt_pk_bf16_f32 v139, v114, v115\n\t"
    "v_mfma_f32_16x16x32_bf16 v[100:103], v[172:175], v[200:203], v[148:151]\n\t"
    "v_exp_f32 v116, v116\n\t"
    "v_mfma_f32_16x16x32_bf16 v[100:103], v[232:235], v[204:207], v[100:103]\n\t"
    "v_exp_f32 v117, v117\n\t"
    "v_mfma_f32_16x16x32_bf16 v[104:107], v[172:175], v[216:219], v[152:155]\n\t"
    "ds_read_b128 v[172:175], v253 offset:6144\n\t"
    "v_cvt_pk_bf16_f32 v142, v116, v117\n\t"
    "v_mfma_f32_16x16x32_bf16 v[104:107], v[232:235], v[220:223], v[104:107]\n\t"
    "s_waitcnt lgkmcnt(4)\n\t"
    "ds_read_b64_tr_b16 v[232:233], v248 offset:24576\n\t"
    "v_mfma_f32_16x16x32_bf16 v[96:99], v[236:239], v[192:195], v[96:99]\n\t"
    "v_exp_f32 v118, v118\n\t"
    "v_mfma_f32_16x16x32_bf16 v[96:99], v[240:243], v[196:199], v[96:99]\n\t"
    "v_exp_f32 v119, v119\n\t"
    "v_mfma_f32_16x16x32_bf16 v[100:103], v[236:239], v[208:211], v[100:103]\n\t"
    "ds_read_b64_tr_b16 v[234:235], v248 offset:26624\n\t"
    "v_cvt_pk_bf16_f32 v143, v118, v119\n\t"
    "v_mfma_f32_16x16x32_bf16 v[100:103], v[240:243], v[212:215], v[100:103]\n\t"
    "v_mfma_f32_16x16x32_bf16 v[104:107], v[236:239], v[224:227], v[104:107]\n\t"
    "ds_read_b64_tr_b16 v[236:237], v249 offset:24576\n\t"
    "ds_read_b64_tr_b16 v[238:239], v249 offset:26624\n\t"
    "v_mfma_f32_16x16x32_bf16 v[104:107], v[240:243], v[228:231], v[104:107]\n\t"
    "s_waitcnt lgkmcnt(6)\n\t"
    "ds_read_b64_tr_b16 v[240:241], v250 offset:24576\n\t"
    "v_mfma_f32_16x16x32_bf16 v[108:111], v[176:179], v[184:187], v[144:147]\n\t"
    "v_exp_f32 v96, v96\n\t"
    "v_mfma_f32_16x16x32_bf16 v[108:111], v[244:247], v[188:191], v[108:111]\n\t"
    "v_exp_f32 v97, v97\n\t"
    "v_mfma_f32_16x16x32_bf16 v[112:115], v[176:179], v[200:203], v[148:151]\n\t"
    "ds_read_b64_tr_b16 v[242:243], v250 offset:26624\n\t"
    "v_cvt_pk_bf16_f32 v120, v96, v97\n\t"
    "v_mfma_f32_16x16x32_bf16 v[112:115], v[244:247], v[204:207], v[112:115]\n\t"
    "v_exp_f32 v98, v98\n\t"
    "v_mfma_f32_16x16x32_bf16 v[116:119], v[176:179], v[216:219], v[152:155]\n\t"
    "v_exp_f32 v99, v99\n\t"
    "v_mfma_f32_16x16x32_bf16 v[116:119], v[244:247], v[220:223], v[116:119]\n\t"
    "s_waitcnt lgkmcnt(6)\n\t"
    "ds_read_b64_tr_b16 v[244:245], v251 offset:24576\n\t"
    "v_mfma_f32_16x16x32_bf16 v[108:111], v[180:183], v[192:195], v[108:111]\n\t"
    "ds_read_b64_tr_b16 v[246:247], v251 offset:26624\n\t"
    "ds_read_b64_tr_b16 v[176:177], v248 offset:28672\n\t"
    "v_mfma_f32_16x16x32_bf16 v[108:111], v[172:175], v[196:199], v[108:111]\n\t"
    "ds_read_b64_tr_b16 v[178:179], v248 offset:30720\n\t"
    "v_cvt_pk_bf16_f32 v121, v98, v99\n\t"
    "v_mfma_f32_16x16x32_bf16 v[112:115], v[180:183], v[208:211], v[112:115]\n\t"
    "v_exp_f32 v100, v100\n\t"
    "v_mfma_f32_16x16x32_bf16 v[112:115], v[172:175], v[212:215], v[112:115]\n\t"
    "v_exp_f32 v101, v101\n\t"
    "v_mfma_f32_16x16x32_bf16 v[116:119], v[180:183], v[224:227], v[116:119]\n\t"
    "v_exp_f32 v102, v102\n\t"
    "v_mfma_f32_16x16x32_bf16 v[116:119], v[172:175], v[228:231], v[116:119]\n\t"
    "ds_read_b64_tr_b16 v[172:173], v249 offset:28672\n\t"
    "ds_read_b64_tr_b16 v[174:175], v249 offset:30720\n\t"
    "v_mfma_f32_16x16x32_bf16 v[156:159], v[168:171], v[132:135], v[156:159]\n\t"
    "ds_read_b64_tr_b16 v[180:181], v250 offset:28672\n\t"
    "ds_read_b64_tr_b16 v[182:183], v250 offset:30720\n\t"
    "v_mfma_f32_16x16x32_bf16 v[160:163], v[168:171], v[136:139], v[160:163]\n\t"
    "v_exp_f32 v103, v103\n\t"
    "v_mfma_f32_16x16x32_bf16 v[164:167], v[168:171], v[140:143], v[164:167]\n\t"
    "s_waitcnt lgkmcnt(10)\n\t"
    "v_cvt_pk_bf16_f32 v124, v100, v101\n\t"
    "v_mfma_f32_16x16x32_bf16 v[0:3], v[232:235], v[132:135], v[0:3]\n\t"
    "v_exp_f32 v104, v104\n\t"
    "v_mfma_f32_16x16x32_bf16 v[4:7], v[232:235], v[136:139], v[4:7]\n\t"
    "v_exp_f32 v105, v105\n\t"
    "v_mfma_f32_16x16x32_bf16 v[8:11], v[232:235], v[140:143], v[8:11]\n\t"
    "v_cvt_pk_bf16_f32 v125, v102, v103\n\t"
    "v_cvt_pk_bf16_f32 v128, v104, v105\n\t"
    "v_mfma_f32_16x16x32_bf16 v[12:15], v[236:239], v[132:135], v[12:15]\n\t"
    "v_exp_f32 v106, v106\n\t"
    "v_mfma_f32_16x16x32_bf16 v[16:19], v[236:239], v[136:139], v[16:19]\n\t"
    "v_exp_f32 v107, v107\n\t"
    "v_mfma_f32_16x16x32_bf16 v[20:23], v[236:239], v[140:143], v[20:23]\n\t"
    "s_waitcnt lgkmcnt(6)\n\t"
    "ds_read_b64_tr_b16 v[236:237], v251 offset:28672\n\t"
    "v_mfma_f32_16x16x32_bf16 v[24:27], v[240:243], v[132:135], v[24:27]\n\t"
    "ds_read_b64_tr_b16 v[238:239], v251 offset:30720\n\t"
    "s_barrier\n\t"
    "v_mfma_f32_16x16x32_bf16 v[28:31], v[240:243], v[136:139], v[28:31]\n\t"
    "ds_read_b128 v[232:235], v252 offset:16384\n\t"
    "v_cvt_pk_bf16_f32 v129, v106, v107\n\t"
    "v_mfma_f32_16x16x32_bf16 v[32:35], v[240:243], v[140:143], v[32:35]\n\t"
    "v_exp_f32 v108, v108\n\t"
    "v_mfma_f32_16x16x32_bf16 v[36:39], v[244:247], v[132:135], v[36:39]\n\t"
    "v_exp_f32 v109, v109\n\t"
    "v_mfma_f32_16x16x32_bf16 v[40:43], v[244:247], v[136:139], v[40:43]\n\t"
    "ds_read_b128 v[240:243], v253 offset:16384\n\t"
    "v_cvt_pk_bf16_f32 v122, v108, v109\n\t"
    "v_mfma_f32_16x16x32_bf16 v[44:47], v[244:247], v[140:143], v[44:47]\n\t"
    "s_waitcnt lgkmcnt(6)\n\t"
    "ds_read_b128 v[244:247], v252 offset:18432\n\t"
    "v_mfma_f32_16x16x32_bf16 v[48:51], v[176:179], v[132:135], v[48:51]\n\t"
    "v_exp_f32 v110, v110\n\t"
    "v_mfma_f32_16x16x32_bf16 v[52:55], v[176:179], v[136:139], v[52:55]\n\t"
    "v_exp_f32 v111, v111\n\t"
    "v_mfma_f32_16x16x32_bf16 v[56:59], v[176:179], v[140:143], v[56:59]\n\t"
    "v_exp_f32 v112, v112\n\t"
    "v_mfma_f32_16x16x32_bf16 v[60:63], v[172:175], v[132:135], v[60:63]\n\t"
    "v_exp_f32 v113, v113\n\t"
    "v_mfma_f32_16x16x32_bf16 v[64:67], v[172:175], v[136:139], v[64:67]\n\t"
    "v_cvt_pk_bf16_f32 v123, v110, v111\n\t"
    "v_cvt_pk_bf16_f32 v126, v112, v113\n\t"
    "v_mfma_f32_16x16x32_bf16 v[68:71], v[172:175], v[140:143], v[68:71]\n\t"
    "s_waitcnt lgkmcnt(3)\n\t"
    "ds_read_b128 v[172:175], v253 offset:18432\n\t"
    "v_mfma_f32_16x16x32_bf16 v[72:75], v[180:183], v[132:135], v[72:75]\n\t"
    "v_exp_f32 v114, v114\n\t"
    "v_mfma_f32_16x16x32_bf16 v[76:79], v[180:183], v[136:139], v[76:79]\n\t"
    "v_exp_f32 v115, v115\n\t"
    "v_mfma_f32_16x16x32_bf16 v[80:83], v[180:183], v[140:143], v[80:83]\n\t"
    "ds_read_b128 v[180:183], v252 offset:20480\n\t"
    "ds_read_b128 v[176:179], v253 offset:20480\n\t"
    "v_mfma_f32_16x16x32_bf16 v[84:87], v[236:239], v[132:135], v[84:87]\n\t"
    "v_exp_f32 v116, v116\n\t"
    "v_mfma_f32_16x16x32_bf16 v[88:91], v[236:239], v[136:139], v[88:91]\n\t"
    "v_exp_f32 v117, v117\n\t"
    "v_mfma_f32_16x16x32_bf16 v[92:95], v[236:239], v[140:143], v[92:95]\n\t"
    "s_sub_u32 s8, s8, 1\n\t"
    "s_cbranch_scc0 gqa_at_0\n\t"
    "s_waitcnt lgkmcnt(4)\n\t"
    "ds_read_b128 v[236:239], v252 offset:22528\n\t"
    "v_mfma_f32_16x16x32_bf16 v[96:99], v[232:235], v[184:187], v[144:147]\n\t"
    "v_cvt_pk_bf16_f32 v127, v114, v115\n\t"
    "v_cvt_pk_bf16_f32 v130, v116, v117\n\t"
    "v_mfma_f32_16x16x32_bf16 v[96:99], v[240:243], v[188:191], v[96:99]\n\t"
    "v_exp_f32 v118, v118\n\t"
    "v_mfma_f32_16x16x32_bf16 v[100:103], v[232:235], v[200:203], v[148:151]\n\t"
    "v_exp_f32 v119, v119\n\t"
    "v_mfma_f32_16x16x32_bf16 v[100:103], v[240:243], v[204:207], v[100:103]\n\t"
    "v_cvt_pk_bf16_f32 v131, v118, v119\n\t"
    "v_mfma_f32_16x16x32_bf16 v[104:107], v[232:235], v[216:219], v[152:155]\n\t"
    "v_mfma_f32_16x16x32_bf16 v[104:107], v[240:243], v[220:223], v[104:107]\n\t"
    "s_waitcnt lgkmcnt(3)\n\t"
    "ds_read_b128 v[240:243], v253 offset:22528\n\t"
    "v_mfma_f32_16x16x32_bf16 v[96:99], v[244:247], v[192:195], v[96:99]\n\t"
    "v_add_u32 v248, s11, v248\n\t"
    "v_add_u32 v249, s11, v249\n\t"
    "v_mfma_f32_16x16x32_bf16 v[96:99], v[172:175], v[196:199], v[96:99]\n\t"
    "v_add_u32 v250, s11, v250\n\t"
    "v_add_u32 v251, s11, v251\n\t"
    "v_mfma_f32_16x16x32_bf16 v[100:103], v[244:247], v[208:211], v[100:103]\n\t"
    "ds_read_b64_tr_b16 v[232:233], v248 offset:8192\n\t"
    "ds_read_b64_tr_b16 v[234:235], v248 offset:10240\n\t"
    "v_mfma_f32_16x16x32_bf16 v[100:103], v[172:175], v[212:215], v[100:103]\n\t"
    "v_mfma_f32_16x16x32_bf16 v[104:107], v[244:247], v[224:227], v[104:107]\n\t"
    "ds_read_b64_tr_b16 v[244:245], v249 offset:8192\n\t"
    "ds_read_b64_tr_b16 v[246:247], v249 offset:10240\n\t"
    "v_mfma_f32_16x16x32_bf16 v[104:107], v[172:175], v[228:231], v[104:107]\n\t"
    "s_waitcnt lgkmcnt(6)\n\t"
    "ds_read_b64_tr_b16 v[172:173], v250 offset:8192\n\t"
    "v_mfma_f32_16x16x32_bf16 v[108:111], v[180:183], v[184:187], v[144:147]\n\t"
    "v_exp_f32 v96, v96\n\t"
    "v_mfma_f32_16x16x32_bf16 v[108:111], v[176:179], v[188:191], v[108:111]\n\t"
    "v_exp_f32 v97, v97\n\t"
    "v_mfma_f32_16x16x32_bf16 v[112:115], v[180:183], v[200:203], v[148:151]\n\t"
    "ds_read_b64_tr_b16 v[174:175], v250 offset:10240\n\t"
    "v_cvt_pk_bf16_f32 v132, v96, v97\n\t"
    "v_mfma_f32_16x16x32_bf16 v[112:115], v[176:179], v[204:207], v[112:115]\n\t"
    "v_exp_f32 v98, v98\n\t"
    "v_mfma_f32_16x16x32_bf16 v[116:119], v[180:183], v[216:219], v[152:155]\n\t"
    "v_exp_f32 v99, v99\n\t"
    "v_mfma_f32_16x16x32_bf16 v[116:119], v[176:179], v[220:223], v[116:119]\n\t"
    "s_waitcnt lgkmcnt(6)\n\t"
    "ds_read_b64_tr_b16 v[176:177], v251 offset:8192\n\t"
    "v_mfma_f32_16x16x32_bf16 v[108:111], v[236:239], v[192:195], v[108:111]\n\t"
    "ds_read_b64_tr_b16 v[178:179], v251 offset:10240\n\t"
    "v_cvt_pk_bf16_f32 v133, v98, v99\n\t"
    "v_mfma_f32_16x16x32_bf16 v[108:111], v[240:243], v[196:199], v[108:111]\n\t"
    "v_exp_f32 v100, v100\n\t"
    "v_mfma_f32_16x16x32_bf16 v[112:115], v[236:239], v[208:211], v[112:115]\n\t"
    "v_exp_f32 v101, v101\n\t"
    "v_mfma_f32_16x16x32_bf16 v[112:115], v[240:243], v[212:215], v[112:115]\n\t"
    "v_exp_f32 v102, v102\n\t"
    "v_mfma_f32_16x16x32_bf16 v[116:119], v[236:239], v[224:227], v[116:119]\n\t"
    "ds_read_b64_tr_b16 v[236:237], v248 offset:12288\n\t"
    "ds_read_b64_tr_b16 v[238:239], v248 offset:14336\n\t"
    "v_mfma_f32_16x16x32_bf16 v[116:119], v[240:243], v[228:231], v[116:119]\n\t"
    "ds_read_b64_tr_b16 v[240:241], v249 offset:12288\n\t"
    "ds_read_b64_tr_b16 v[242:243], v249 offset:14336\n\t"
    "v_mfma_f32_16x16x32_bf16 v[156:159], v[168:171], v[120:123], v[156:159]\n\t"
    "ds_read_b64_tr_b16 v[180:181], v250 offset:12288\n\t"
    "ds_read_b64_tr_b16 v[182:183], v250 offset:14336\n\t"
    "v_mfma_f32_16x16x32_bf16 v[160:163], v[168:171], v[124:127], v[160:163]\n\t"
    "v_exp_f32 v108, v108\n\t"
    "v_mfma_f32_16x16x32_bf16 v[164:167], v[168:171], v[128:131], v[164:167]\n\t"
    "s_waitcnt lgkmcnt(10)\n\t"
    "v_cvt_pk_bf16_f32 v136, v100, v101\n\t"
    "v_mfma_f32_16x16x32_bf16 v[0:3], v[232:235], v[120:123], v[0:3]\n\t"
    "v_exp_f32 v109, v109\n\t"
    "v_mfma_f32_16x16x32_bf16 v[4:7], v[232:235], v[124:127], v[4:7]\n\t"
    "v_exp_f32 v110, v110\n\t"
    "v_mfma_f32_16x16x32_bf16 v[8:11], v[232:235], v[128:131], v[8:11]\n\t"
    "v_exp_f32 v111, v111\n\t"
    "v_mfma_f32_16x16x32_bf16 v[12:15], v[244:247], v[120:123], v[12:15]\n\t"
    "v_exp_f32 v103, v103\n\t"
    "v_mfma_f32_16x16x32_bf16 v[16:19], v[244:247], v[124:127], v[16:19]\n\t"
    "v_cvt_pk_bf16_f32 v134, v108, v109\n\t"
    "v_cvt_pk_bf16_f32 v135, v110, v111\n\t"
    "v_mfma_f32_16x16x32_bf16 v[20:23], v[244:247], v[128:131], v[20:23]\n\t"
    "s_waitcnt lgkmcnt(6)\n\t"
    "ds_read_b64_tr_b16 v[244:245], v251 offset:12288\n\t"
    "v_mfma_f32_16x16x32_bf16 v[24:27], v[172:175], v[120:123], v[24:27]\n\t"
    "ds_read_b64_tr_b16 v[246:247], v251 offset:14336\n\t"
    "v_cvt_pk_bf16_f32 v137, v102, v103\n\t"
    "v_mfma_f32_16x16x32_bf16 v[28:31], v[172:175], v[124:127], v[28:31]\n\t"
    "v_exp_f32 v112, v112\n\t"
    "v_mfma_f32_16x16x32_bf16 v[32:35], v[172:175], v[128:131], v[32:35]\n\t"
    "ds_read_b64_tr_b16 v[172:173], v248 offset:24576\n\t"
    "ds_read_b64_tr_b16 v[174:175], v248 offset:26624\n\t"
    "v_mfma_f32_16x16x32_bf16 v[36:39], v[176:179], v[120:123], v[36:39]\n\t"
    "ds_read_b64_tr_b16 v[232:233], v249 offset:24576\n\t"
    "ds_read_b64_tr_b16 v[234:235], v249 offset:26624\n\t"
    "v_mfma_f32_16x16x32_bf16 v[40:43], v[176:179], v[124:127], v[40:43]\n\t"
    "v_exp_f32 v113, v113\n\t"
    "v_mfma_f32_16x16x32_bf16 v[44:47], v[176:179], v[128:131], v[44:47]\n\t"
    "s_waitcnt lgkmcnt(8)\n\t"
    "v_cvt_pk_bf16_f32 v138, v112, v113\n\t"
    "v_mfma_f32_16x16x32_bf16 v[48:51], v[236:239], v[120:123], v[48:51]\n\t"
    "v_exp_f32 v114, v114\n\t"
    "v_mfma_f32_16x16x32_bf16 v[52:55], v[236:239], v[124:127], v[52:55]\n\t"
    "v_exp_f32 v115, v115\n\t"
    "v_mfma_f32_16x16x32_bf16 v[56:59], v[236:239], v[128:131], v[56:59]\n\t"
    "ds_read_b64_tr_b16 v[236:237], v250 offset:24576\n\t"
    "ds_read_b64_tr_b16 v[238:239], v250 offset:26624\n\t"
    "v_mfma_f32_16x16x32_bf16 v[60:63], v[240:243], v[120:123], v[60:63]\n\t"
    "v_exp_f32 v104, v104\n\t"
    "v_mfma_f32_16x16x32_bf16 v[64:67], v[240:243], v[124:127], v[64:67]\n\t"
    "v_exp_f32 v105, v105\n\t"
    "v_mfma_f32_16x16x32_bf16 v[68:71], v[240:243], v[128:131], v[68:71]\n\t"
    "s_waitcnt lgkmcnt(6)\n\t"
    "ds_read_b64_tr_b16 v[240:241], v251 offset:24576\n\t"
    "v_mfma_f32_16x16x32_bf16 v[72:75], v[180:183], v[120:123], v[72:75]\n\t"
    "ds_read_b64_tr_b16 v[242:243], v251 offset:26624\n\t"
    "v_cvt_pk_bf16_f32 v139, v114, v115\n\t"
    "v_mfma_f32_16x16x32_bf16 v[76:79], v[180:183], v[124:127], v[76:79]\n\t"
    "v_exp_f32 v106, v106\n\t"
    "v_mfma_f32_16x16x32_bf16 v[80:83], v[180:183], v[128:131], v[80:83]\n\t"
    "v_exp_f32 v107, v107\n\t"
    "v_mfma_f32_16x16x32_bf16 v[84:87], v[244:247], v[120:123], v[84:87]\n\t"
    "v_exp_f32 v116, v116\n\t"
    "v_mfma_f32_16x16x32_bf16 v[88:91], v[244:247], v[124:127], v[88:91]\n\t"
    "v_exp_f32 v117, v117\n\t"
    "v_mfma_f32_16x16x32_bf16 v[92:95], v[244:247], v[128:131], v[92:95]\n\t"
    "v_exp_f32 v118, v118\n\t"
    "v_mfma_f32_16x16x32_bf16 v[156:159], v[168:171], v[132:135], v[156:159]\n\t"
    "v_exp_f32 v119, v119\n\t"
    "v_mfma_f32_16x16x32_bf16 v[160:163], v[168:171], v[136:139], v[160:163]\n\t"
    "v_cvt_pk_bf16_f32 v140, v104, v105\n\t"
    "v_cvt_pk_bf16_f32 v141, v106, v107\n\t"
    "v_cvt_pk_bf16_f32 v142, v116, v117\n\t"
    "v_cvt_pk_bf16_f32 v143, v118, v119\n\t"
    "s_barrier\n\t"
    "s_nop 0\n\t"
    "v_mfma_f32_16x16x32_bf16 v[164:167], v[168:171], v[140:143], v[164:167]\n\t"
    "s_waitcnt lgkmcnt(4)\n\t"
    "ds_read_b64_tr_b16 v[176:177], v248 offset:28672\n\t"
    "v_mfma_f32_16x16x32_bf16 v[0:3], v[172:175], v[132:135], v[0:3]\n\t"
    "ds_read_b64_tr_b16 v[178:179], v248 offset:30720\n\t"
    "ds_read_b64_tr_b16 v[244:245], v249 offset:28672\n\t"
    "v_mfma_f32_16x16x32_bf16 v[4:7], v[172:175], v[136:139], v[4:7]\n\t"
    "ds_read_b64_tr_b16 v[246:247], v249 offset:30720\n\t"
    "ds_read_b64_tr_b16 v[180:181], v250 offset:28672\n\t"
    "v_mfma_f32_16x16x32_bf16 v[8:11], v[172:175], v[140:143], v[8:11]\n\t"
    "ds_read_b64_tr_b16 v[182:183], v250 offset:30720\n\t"
    "ds_read_b64_tr_b16 v[172:173], v251 offset:28672\n\t"
    "v_mfma_f32_16x16x32_bf16 v[12:15], v[232:235], v[132:135], v[12:15]\n\t"
    "ds_read_b64_tr_b16 v[174:175], v251 offset:30720\n\t"
    "v_xor_b32 v250, s61, v248\n\t"
    "v_mfma_f32_16x16x32_bf16 v[16:19], v[232:235], v[136:139], v[16:19]\n\t"
    "v_rcp_f32 v144, v156\n\t"
    "v_mfma_f32_16x16x32_bf16 v[20:23], v[232:235], v[140:143], v[20:23]\n\t"
    "s_waitcnt lgkmcnt(8)\n\t"
    "v_xor_b32 v251, s61, v249\n\t"
    "v_mfma_f32_16x16x32_bf16 v[24:27], v[236:239], v[132:135], v[24:27]\n\t"
    "v_rcp_f32 v145, v160\n\t"
    "v_mfma_f32_16x16x32_bf16 v[28:31], v[236:239], v[136:139], v[28:31]\n\t"
    "v_rcp_f32 v146, v164\n\t"
    "v_mfma_f32_16x16x32_bf16 v[32:35], v[236:239], v[140:143], v[32:35]\n\t"
    "v_xor_b32 v248, s60, v248\n\t"
    "v_xor_b32 v249, s60, v249\n\t"
    "v_mfma_f32_16x16x32_bf16 v[36:39], v[240:243], v[132:135], v[36:39]\n\t"
    "v_pk_mul_f32 v[0:1], v[0:1], v[144:145] op_sel:[0,0] op_sel_hi:[1,0]\n\t"
    "v_pk_mul_f32 v[2:3], v[2:3], v[144:145] op_sel:[0,0] op_sel_hi:[1,0]\n\t"
    "v_pk_mul_f32 v[12:13], v[12:13], v[144:145] op_sel:[0,0] op_sel_hi:[1,0]\n\t"
    "v_pk_mul_f32 v[14:15], v[14:15], v[144:145] op_sel:[0,0] op_sel_hi:[1,0]\n\t"
    "v_pk_mul_f32 v[4:5], v[4:5], v[144:145] op_sel:[0,1] op_sel_hi:[1,1]\n\t"
    "v_pk_mul_f32 v[6:7], v[6:7], v[144:145] op_sel:[0,1] op_sel_hi:[1,1]\n\t"
    "v_pk_mul_f32 v[16:17], v[16:17], v[144:145] op_sel:[0,1] op_sel_hi:[1,1]\n\t"
    "v_pk_mul_f32 v[18:19], v[18:19], v[144:145] op_sel:[0,1] op_sel_hi:[1,1]\n\t"
    "v_pk_mul_f32 v[8:9], v[8:9], v[146:147] op_sel:[0,0] op_sel_hi:[1,0]\n\t"
    "v_pk_mul_f32 v[10:11], v[10:11], v[146:147] op_sel:[0,0] op_sel_hi:[1,0]\n\t"
    "v_pk_mul_f32 v[20:21], v[20:21], v[146:147] op_sel:[0,0] op_sel_hi:[1,0]\n\t"
    "v_pk_mul_f32 v[22:23], v[22:23], v[146:147] op_sel:[0,0] op_sel_hi:[1,0]\n\t"
    "v_mfma_f32_16x16x32_bf16 v[40:43], v[240:243], v[136:139], v[40:43]\n\t"
    "ds_read_b64_tr_b16 v[152:153], v248 offset:0\n\t"
    "ds_read_b64_tr_b16 v[154:155], v248 offset:2048\n\t"
    "v_mfma_f32_16x16x32_bf16 v[44:47], v[240:243], v[140:143], v[44:47]\n\t"
    "s_waitcnt lgkmcnt(6)\n\t"
    "ds_read_b64_tr_b16 v[232:233], v249 offset:0\n\t"
    "v_mfma_f32_16x16x32_bf16 v[48:51], v[176:179], v[132:135], v[48:51]\n\t"
    "ds_read_b64_tr_b16 v[234:235], v249 offset:2048\n\t"
    "v_add_f32 v148, v156, v160\n\t"
    "v_mfma_f32_16x16x32_bf16 v[52:55], v[176:179], v[136:139], v[52:55]\n\t"
    "v_add_f32 v148, v148, v164\n\t"
    "ds_read_b64_tr_b16 v[156:157], v248 offset:32768\n\t"
    "v_mfma_f32_16x16x32_bf16 v[56:59], v[176:179], v[140:143], v[56:59]\n\t"
    "ds_read_b64_tr_b16 v[158:159], v248 offset:34816\n\t"
    "ds_read_b64_tr_b16 v[160:161], v249 offset:32768\n\t"
    "v_mfma_f32_16x16x32_bf16 v[60:63], v[244:247], v[132:135], v[60:63]\n\t"
    "v_pk_mul_f32 v[24:25], v[24:25], v[144:145] op_sel:[0,0] op_sel_hi:[1,0]\n\t"
    "v_pk_mul_f32 v[26:27], v[26:27], v[144:145] op_sel:[0,0] op_sel_hi:[1,0]\n\t"
    "v_pk_mul_f32 v[36:37], v[36:37], v[144:145] op_sel:[0,0] op_sel_hi:[1,0]\n\t"
    "v_pk_mul_f32 v[38:39], v[38:39], v[144:145] op_sel:[0,0] op_sel_hi:[1,0]\n\t"
    "v_pk_mul_f32 v[28:29], v[28:29], v[144:145] op_sel:[0,1] op_sel_hi:[1,1]\n\t"
    "v_pk_mul_f32 v[30:31], v[30:31], v[144:145] op_sel:[0,1] op_sel_hi:[1,1]\n\t"
    "v_pk_mul_f32 v[40:41], v[40:41], v[144:145] op_sel:[0,1] op_sel_hi:[1,1]\n\t"
    "v_pk_mul_f32 v[42:43], v[42:43], v[144:145] op_sel:[0,1] op_sel_hi:[1,1]\n\t"
    "v_pk_mul_f32 v[32:33], v[32:33], v[146:147] op_sel:[0,0] op_sel_hi:[1,0]\n\t"
    "v_pk_mul_f32 v[34:35], v[34:35], v[146:147] op_sel:[0,0] op_sel_hi:[1,0]\n\t"
    "v_pk_mul_f32 v[44:45], v[44:45], v[146:147] op_sel:[0,0] op_sel_hi:[1,0]\n\t"
    "v_pk_mul_f32 v[46:47], v[46:47], v[146:147] op_sel:[0,0] op_sel_hi:[1,0]\n\t"
    "v_mfma_f32_16x16x32_bf16 v[64:67], v[244:247], v[136:139], v[64:67]\n\t"
    "ds_read_b64_tr_b16 v[162:163], v249 offset:34816\n\t"
    "v_cmp_nlt_f32 vcc, v148, s65\n\t"
    "v_mfma_f32_16x16x32_bf16 v[68:71], v[244:247], v[140:143], v[68:71]\n\t"
    "s_waitcnt lgkmcnt(8)\n\t"
    "v_mov_b32 v147, 155648\n\t"
    "v_mfma_f32_16x16x32_bf16 v[72:75], v[180:183], v[132:135], v[72:75]\n\t"
    "s_cmp_lg_u64 vcc, 0\n\t"
    "s_cselect_b32 s39, s62, 0\n\t"
    "v_mfma_f32_16x16x32_bf16 v[76:79], v[180:183], v[136:139], v[76:79]\n\t"
    "v_mov_b32 v148, s39\n\t"
    "v_lshl_add_u32 v149, s54, 2, v147\n\t"
    "v_mfma_f32_16x16x32_bf16 v[80:83], v[180:183], v[140:143], v[80:83]\n\t"
    "v_cvt_pk_bf16_f32 v96, v0, v1\n\t"
    "v_cvt_pk_bf16_f32 v97, v2, v3\n\t"
    "v_mfma_f32_16x16x32_bf16 v[84:87], v[172:175], v[132:135], v[84:87]\n\t"
    "v_pk_mul_f32 v[48:49], v[48:49], v[144:145] op_sel:[0,0] op_sel_hi:[1,0]\n\t"
    "v_pk_mul_f32 v[50:51], v[50:51], v[144:145] op_sel:[0,0] op_sel_hi:[1,0]\n\t"
    "v_pk_mul_f32 v[60:61], v[60:61], v[144:145] op_sel:[0,0] op_sel_hi:[1,0]\n\t"
    "v_pk_mul_f32 v[62:63], v[62:63], v[144:145] op_sel:[0,0] op_sel_hi:[1,0]\n\t"
    "v_pk_mul_f32 v[52:53], v[52:53], v[144:145] op_sel:[0,1] op_sel_hi:[1,1]\n\t"
    "v_pk_mul_f32 v[54:55], v[54:55], v[144:145] op_sel:[0,1] op_sel_hi:[1,1]\n\t"
    "v_pk_mul_f32 v[64:65], v[64:65], v[144:145] op_sel:[0,1] op_sel_hi:[1,1]\n\t"
    "v_pk_mul_f32 v[66:67], v[66:67], v[144:145] op_sel:[0,1] op_sel_hi:[1,1]\n\t"
    "v_pk_mul_f32 v[56:57], v[56:57], v[146:147] op_sel:[0,0] op_sel_hi:[1,0]\n\t"
    "v_pk_mul_f32 v[58:59], v[58:59], v[146:147] op_sel:[0,0] op_sel_hi:[1,0]\n\t"
    "v_pk_mul_f32 v[68:69], v[68:69], v[146:147] op_sel:[0,0] op_sel_hi:[1,0]\n\t"
    "v_pk_mul_f32 v[70:71], v[70:71], v[146:147] op_sel:[0,0] op_sel_hi:[1,0]\n\t"
    "v_mfma_f32_16x16x32_bf16 v[88:91], v[172:175], v[136:139], v[88:91]\n\t"
    "v_cvt_pk_bf16_f32 v98, v12, v13\n\t"
    "v_cvt_pk_bf16_f32 v99, v14, v15\n\t"
    "v_mfma_f32_16x16x32_bf16 v[92:95], v[172:175], v[140:143], v[92:95]\n\t"
    "v_cvt_pk_bf16_f32 v100, v4, v5\n\t"
    "v_cvt_pk_bf16_f32 v101, v6, v7\n\t"
    "v_cvt_pk_bf16_f32 v102, v16, v17\n\t"
    "v_cvt_pk_bf16_f32 v103, v18, v19\n\t"
    "v_cvt_pk_bf16_f32 v104, v8, v9\n\t"
    "v_cvt_pk_bf16_f32 v105, v10, v11\n\t"
    "v_cvt_pk_bf16_f32 v106, v20, v21\n\t"
    "v_cvt_pk_bf16_f32 v107, v22, v23\n\t"
    "s_waitcnt lgkmcnt(4)\n\t"
    "v_mfma_f32_16x16x32_bf16 v[0:3], v[152:155], v[96:99], 0\n\t"
    "ds_write_b128 v254, v[96:99] offset:0\n\t"
    "v_mfma_f32_16x16x32_bf16 v[8:11], v[152:155], v[100:103], 0\n\t"
    "ds_write_b128 v254, v[100:103] offset:1024\n\t"
    "v_mfma_f32_16x16x32_bf16 v[16:19], v[152:155], v[104:107], 0\n\t"
    "ds_write_b128 v254, v[104:107] offset:2048\n\t"
    "v_mfma_f32_16x16x32_bf16 v[4:7], v[232:235], v[96:99], 0\n\t"
    "ds_write_b32 v149, v148\n\t"
    "ds_read_b64_tr_b16 v[164:165], v248 offset:4096\n\t"
    "v_mfma_f32_16x16x32_bf16 v[12:15], v[232:235], v[100:103], 0\n\t"
    "ds_read_b64_tr_b16 v[166:167], v248 offset:6144\n\t"
    "ds_read_b64_tr_b16 v[168:169], v249 offset:4096\n\t"
    "v_mfma_f32_16x16x32_bf16 v[20:23], v[232:235], v[104:107], 0\n\t"
    "v_cvt_pk_bf16_f32 v108, v24, v25\n\t"
    "v_cvt_pk_bf16_f32 v109, v26, v27\n\t"
    "v_cvt_pk_bf16_f32 v110, v36, v37\n\t"
    "v_cvt_pk_bf16_f32 v111, v38, v39\n\t"
    "v_cvt_pk_bf16_f32 v112, v28, v29\n\t"
    "v_cvt_pk_bf16_f32 v113, v30, v31\n\t"
    "v_cvt_pk_bf16_f32 v114, v40, v41\n\t"
    "v_cvt_pk_bf16_f32 v115, v42, v43\n\t"
    "v_cvt_pk_bf16_f32 v116, v32, v33\n\t"
    "v_cvt_pk_bf16_f32 v117, v34, v35\n\t"
    "v_cvt_pk_bf16_f32 v118, v44, v45\n\t"
    "v_cvt_pk_bf16_f32 v119, v46, v47\n\t"
    "s_waitcnt lgkmcnt(7)\n\t"
    "v_mfma_f32_16x16x32_bf16 v[24:27], v[156:159], v[96:99], 0\n\t"
    "ds_read_b64_tr_b16 v[170:171], v249 offset:6144\n\t"
    "v_pk_mul_f32 v[72:73], v[72:73], v[144:145] op_sel:[0,0] op_sel_hi:[1,0]\n\t"
    "v_pk_mul_f32 v[74:75], v[74:75], v[144:145] op_sel:[0,0] op_sel_hi:[1,0]\n\t"
    "v_pk_mul_f32 v[84:85], v[84:85], v[144:145] op_sel:[0,0] op_sel_hi:[1,0]\n\t"
    "v_pk_mul_f32 v[86:87], v[86:87], v[144:145] op_sel:[0,0] op_sel_hi:[1,0]\n\t"
    "v_pk_mul_f32 v[76:77], v[76:77], v[144:145] op_sel:[0,1] op_sel_hi:[1,1]\n\t"
    "v_pk_mul_f32 v[78:79], v[78:79], v[144:145] op_sel:[0,1] op_sel_hi:[1,1]\n\t"
    "v_pk_mul_f32 v[88:89], v[88:89], v[144:145] op_sel:[0,1] op_sel_hi:[1,1]\n\t"
    "v_pk_mul_f32 v[90:91], v[90:91], v[144:145] op_sel:[0,1] op_sel_hi:[1,1]\n\t"
    "v_pk_mul_f32 v[80:81], v[80:81], v[146:147] op_sel:[0,0] op_sel_hi:[1,0]\n\t"
    "v_pk_mul_f32 v[82:83], v[82:83], v[146:147] op_sel:[0,0] op_sel_hi:[1,0]\n\t"
    "v_pk_mul_f32 v[92:93], v[92:93], v[146:147] op_sel:[0,0] op_sel_hi:[1,0]\n\t"
    "v_pk_mul_f32 v[94:95], v[94:95], v[146:147] op_sel:[0,0] op_sel_hi:[1,0]\n\t"
    "v_mfma_f32_16x16x32_bf16 v[32:35], v[156:159], v[100:103], 0\n\t"
    "ds_write_b128 v254, v[108:111] offset:3072\n\t"
    "v_mfma_f32_16x16x32_bf16 v[40:43], v[156:159], v[104:107], 0\n\t"
    "ds_write_b128 v254, v[112:115] offset:4096\n\t"
    "v_mfma_f32_16x16x32_bf16 v[28:31], v[160:163], v[96:99], 0\n\t"
    "ds_write_b128 v254, v[116:119] offset:5120\n\t"
    "v_mfma_f32_16x16x32_bf16 v[36:39], v[160:163], v[100:103], 0\n\t"
    "ds_read_b64_tr_b16 v[236:237], v248 offset:36864\n\t"
    "ds_read_b64_tr_b16 v[238:239], v248 offset:38912\n\t"
    "v_mfma_f32_16x16x32_bf16 v[44:47], v[160:163], v[104:107], 0\n\t"
    "s_waitcnt lgkmcnt(5)\n\t"
    "ds_read_b64_tr_b16 v[240:241], v249 offset:36864\n\t"
    "v_mfma_f32_16x16x32_bf16 v[0:3], v[164:167], v[108:111], v[0:3]\n\t"
    "ds_read_b64_tr_b16 v[242:243], v249 offset:38912\n\t"
    "ds_read_b64_tr_b16 v[176:177], v248 offset:8192\n\t"
    "v_mfma_f32_16x16x32_bf16 v[8:11], v[164:167], v[112:115], v[8:11]\n\t"
    "ds_read_b64_tr_b16 v[178:179], v248 offset:10240\n\t"
    "ds_read_b64_tr_b16 v[244:245], v249 offset:8192\n\t"
    "v_mfma_f32_16x16x32_bf16 v[16:19], v[164:167], v[116:119], v[16:19]\n\t"
    "ds_read_b64_tr_b16 v[246:247], v249 offset:10240\n\t"
    "ds_read_b64_tr_b16 v[180:181], v248 offset:40960\n\t"
    "v_mfma_f32_16x16x32_bf16 v[4:7], v[168:171], v[108:111], v[4:7]\n\t"
    "ds_read_b64_tr_b16 v[182:183], v248 offset:43008\n\t"
    "ds_read_b64_tr_b16 v[172:173], v249 offset:40960\n\t"
    "v_mfma_f32_16x16x32_bf16 v[12:15], v[168:171], v[112:115], v[12:15]\n\t"
    "ds_read_b64_tr_b16 v[174:175], v249 offset:43008\n\t"
    "v_subrev_u32 v252, s67, v254\n\t"
    "v_mfma_f32_16x16x32_bf16 v[20:23], v[168:171], v[116:119], v[20:23]\n\t"
    "s_waitcnt lgkmcnt(8)\n\t"
    "v_add_u32 v253, s63, v252\n\t"
    "v_mfma_f32_16x16x32_bf16 v[24:27], v[236:239], v[108:111], v[24:27]\n\t"
    "v_cvt_pk_bf16_f32 v120, v48, v49\n\t"
    "v_cvt_pk_bf16_f32 v121, v50, v51\n\t"
    "v_mfma_f32_16x16x32_bf16 v[32:35], v[236:239], v[112:115], v[32:35]\n\t"
    "v_cvt_pk_bf16_f32 v122, v60, v61\n\t"
    "v_cvt_pk_bf16_f32 v123, v62, v63\n\t"
    "v_mfma_f32_16x16x32_bf16 v[40:43], v[236:239], v[116:119], v[40:43]\n\t"
    "ds_write_b128 v254, v[120:123] offset:6144\n\t"
    "v_mfma_f32_16x16x32_bf16 v[28:31], v[240:243], v[108:111], v[28:31]\n\t"
    "v_cvt_pk_bf16_f32 v124, v52, v53\n\t"
    "v_cvt_pk_bf16_f32 v125, v54, v55\n\t"
    "v_mfma_f32_16x16x32_bf16 v[36:39], v[240:243], v[112:115], v[36:39]\n\t"
    "v_cvt_pk_bf16_f32 v126, v64, v65\n\t"
    "v_cvt_pk_bf16_f32 v127, v66, v67\n\t"
    "v_mfma_f32_16x16x32_bf16 v[44:47], v[240:243], v[116:119], v[44:47]\n\t"
    "v_cvt_pk_bf16_f32 v128, v56, v57\n\t"
    "v_cvt_pk_bf16_f32 v129, v58, v59\n\t"
    "v_cvt_pk_bf16_f32 v130, v68, v69\n\t"
    "v_cvt_pk_bf16_f32 v131, v70, v71\n\t"
    "s_waitcnt lgkmcnt(5)\n\t"
    "v_mfma_f32_16x16x32_bf16 v[0:3], v[176:179], v[120:123], v[0:3]\n\t"
    "ds_write_b128 v254, v[124:127] offset:7168\n\t"
    "v_mfma_f32_16x16x32_bf16 v[8:11], v[176:179], v[124:127], v[8:11]\n\t"
    "ds_write_b128 v254, v[128:131] offset:8192\n\t"
    "v_mfma_f32_16x16x32_bf16 v[16:19], v[176:179], v[128:131], v[16:19]\n\t"
    "v_cvt_pk_bf16_f32 v132, v72, v73\n\t"
    "v_cvt_pk_bf16_f32 v133, v74, v75\n\t"
    "v_mfma_f32_16x16x32_bf16 v[4:7], v[244:247], v[120:123], v[4:7]\n\t"
    "v_cvt_pk_bf16_f32 v134, v84, v85\n\t"
    "v_cvt_pk_bf16_f32 v135, v86, v87\n\t"
    "v_mfma_f32_16x16x32_bf16 v[12:15], v[244:247], v[124:127], v[12:15]\n\t"
    "ds_write_b128 v254, v[132:135] offset:9216\n\t"
    "v_mfma_f32_16x16x32_bf16 v[20:23], v[244:247], v[128:131], v[20:23]\n\t"
    "v_cvt_pk_bf16_f32 v136, v76, v77\n\t"
    "v_cvt_pk_bf16_f32 v137, v78, v79\n\t"
    "v_cvt_pk_bf16_f32 v138, v88, v89\n\t"
    "v_cvt_pk_bf16_f32 v139, v90, v91\n\t"
    "v_cvt_pk_bf16_f32 v140, v80, v81\n\t"
    "v_cvt_pk_bf16_f32 v141, v82, v83\n\t"
    "v_cvt_pk_bf16_f32 v142, v92, v93\n\t"
    "v_cvt_pk_bf16_f32 v143, v94, v95\n\t"
    "s_waitcnt lgkmcnt(4)\n\t"
    "v_mfma_f32_16x16x32_bf16 v[24:27], v[180:183], v[120:123], v[24:27]\n\t"
    "ds_read_b64_tr_b16 v[152:153], v248 offset:12288\n\t"
    "ds_read_b64_tr_b16 v[154:155], v248 offset:14336\n\t"
    "ds_read_b64_tr_b16 v[232:233], v249 offset:12288\n\t"
    "ds_read_b64_tr_b16 v[234:235], v249 offset:14336\n\t"
    "v_mfma_f32_16x16x32_bf16 v[32:35], v[180:183], v[124:127], v[32:35]\n\t"
    "ds_write_b128 v254, v[136:139] offset:10240\n\t"
    "ds_write_b128 v254, v[140:143] offset:11264\n\t"
    "v_mfma_f32_16x16x32_bf16 v[40:43], v[180:183], v[128:131], v[40:43]\n\t"
    "s_waitcnt lgkmcnt(0)\n\t"
    "s_barrier\n\t"
    "v_mfma_f32_16x16x32_bf16 v[28:31], v[172:175], v[120:123], v[28:31]\n\t"
    "ds_read_b128 v[148:151], v147\n\t"
    "ds_read_b128 v[48:51], v255 offset:0\n\t"
    "v_mfma_f32_16x16x32_bf16 v[36:39], v[172:175], v[124:127], v[36:39]\n\t"
    "ds_read_b128 v[52:55], v255 offset:1024\n\t"
    "ds_read_b128 v[56:59], v255 offset:2048\n\t"
    "v_mfma_f32_16x16x32_bf16 v[44:47], v[172:175], v[128:131], v[44:47]\n\t"
    "ds_read_b128 v[60:63], v255 offset:3072\n\t"
    "v_mfma_f32_16x16x32_bf16 v[0:3], v[152:155], v[132:135], v[0:3]\n\t"
    "ds_read_b64_tr_b16 v[156:157], v248 offset:45056\n\t"
    "ds_read_b64_tr_b16 v[158:159], v248 offset:47104\n\t"
    "ds_read_b64_tr_b16 v[160:161], v249 offset:45056\n\t"
    "ds_read_b64_tr_b16 v[162:163], v249 offset:47104\n\t"
    "v_mfma_f32_16x16x32_bf16 v[8:11], v[152:155], v[136:139], v[8:11]\n\t"
    "ds_read_b128 v[64:67], v255 offset:4096\n\t"
    "ds_read_b128 v[68:71], v255 offset:5120\n\t"
    "v_mfma_f32_16x16x32_bf16 v[16:19], v[152:155], v[140:143], v[16:19]\n\t"
    "ds_read_b128 v[72:75], v255 offset:6144\n\t"
    "ds_read_b128 v[76:79], v255 offset:7168\n\t"
    "v_mfma_f32_16x16x32_bf16 v[4:7], v[232:235], v[132:135], v[4:7]\n\t"
    "ds_read_b128 v[80:83], v255 offset:8192\n\t"
    "ds_read_b128 v[84:87], v255 offset:9216\n\t"
    "v_mfma_f32_16x16x32_bf16 v[12:15], v[232:235], v[136:139], v[12:15]\n\t"
    "ds_read_b128 v[88:91], v255 offset:10240\n\t"
    "ds_read_b128 v[92:95], v255 offset:11264\n\t"
    "v_mfma_f32_16x16x32_bf16 v[20:23], v[232:235], v[140:143], v[20:23]\n\t"
    "s_waitcnt lgkmcnt(8)\n\t"
    "ds_read_b64_tr_b16 v[164:165], v250 offset:0\n\t"
    "v_mfma_f32_16x16x32_bf16 v[24:27], v[156:159], v[132:135], v[24:27]\n\t"
    "ds_read_b64_tr_b16 v[166:167], v250 offset:2048\n\t"
    "ds_read_b64_tr_b16 v[168:169], v250 offset:4096\n\t"
    "ds_read_b64_tr_b16 v[170:171], v250 offset:6144\n\t"
    "v_mfma_f32_16x16x32_bf16 v[32:35], v[156:159], v[136:139], v[32:35]\n\t"
    "ds_read_b64_tr_b16 v[236:237], v251 offset:0\n\t"
    "ds_read_b64_tr_b16 v[238:239], v251 offset:2048\n\t"
    "v_mfma_f32_16x16x32_bf16 v[40:43], v[156:159], v[140:143], v[40:43]\n\t"
    "ds_read_b64_tr_b16 v[240:241], v251 offset:4096\n\t"
    "ds_read_b64_tr_b16 v[242:243], v251 offset:6144\n\t"
    "v_mfma_f32_16x16x32_bf16 v[28:31], v[160:163], v[132:135], v[28:31]\n\t"
    "ds_read_b64_tr_b16 v[176:177], v250 offset:8192\n\t"
    "ds_read_b64_tr_b16 v[178:179], v250 offset:10240\n\t"
    "v_mfma_f32_16x16x32_bf16 v[36:39], v[160:163], v[136:139], v[36:39]\n\t"
    "ds_read_b64_tr_b16 v[244:245], v250 offset:12288\n\t"
    "ds_read_b64_tr_b16 v[246:247], v250 offset:14336\n\t"
    "v_mfma_f32_16x16x32_bf16 v[44:47], v[160:163], v[140:143], v[44:47]\n\t"
    "s_waitcnt lgkmcnt(8)\n\t"
    "v_mfma_f32_16x16x32_bf16 v[0:3], v[164:167], v[48:51], v[0:3]\n\t"
    "ds_read_b64_tr_b16 v[180:181], v251 offset:8192\n\t"
    "v_mfma_f32_16x16x32_bf16 v[0:3], v[168:171], v[60:63], v[0:3]\n\t"
    "ds_read_b64_tr_b16 v[182:183], v251 offset:10240\n\t"
    "ds_read_b64_tr_b16 v[172:173], v251 offset:12288\n\t"
    "v_mfma_f32_16x16x32_bf16 v[8:11], v[164:167], v[52:55], v[8:11]\n\t"
    "ds_read_b64_tr_b16 v[174:175], v251 offset:14336\n\t"
    "ds_read_b64_tr_b16 v[152:153], v250 offset:32768\n\t"
    "v_mfma_f32_16x16x32_bf16 v[8:11], v[168:171], v[64:67], v[8:11]\n\t"
    "ds_read_b64_tr_b16 v[154:155], v250 offset:34816\n\t"
    "ds_read_b64_tr_b16 v[232:233], v250 offset:36864\n\t"
    "v_mfma_f32_16x16x32_bf16 v[16:19], v[164:167], v[56:59], v[16:19]\n\t"
    "ds_read_b64_tr_b16 v[234:235], v250 offset:38912\n\t"
    "ds_read_b64_tr_b16 v[156:157], v251 offset:32768\n\t"
    "v_mfma_f32_16x16x32_bf16 v[16:19], v[168:171], v[68:71], v[16:19]\n\t"
    "s_waitcnt lgkmcnt(13)\n\t"
    "ds_read_b64_tr_b16 v[158:159], v251 offset:34816\n\t"
    "v_mfma_f32_16x16x32_bf16 v[4:7], v[236:239], v[48:51], v[4:7]\n\t"
    "v_or3_b32 v148, v148, v149, v150\n\t"
    "v_or_b32 v148, v148, v151\n\t"
    "v_mfma_f32_16x16x32_bf16 v[4:7], v[240:243], v[60:63], v[4:7]\n\t"
    "v_readfirstlane_b32 s39, v148\n\t"
    "ds_read_b64_tr_b16 v[160:161], v251 offset:36864\n\t"
    "v_mfma_f32_16x16x32_bf16 v[12:15], v[236:239], v[52:55], v[12:15]\n\t"
    "s_cmp_lg_u32 s39, 0\n\t"
    "s_cbranch_scc1 gqa_cfb\n\t"
    "v_mfma_f32_16x16x32_bf16 v[12:15], v[240:243], v[64:67], v[12:15]\n\t"
    "ds_read_b64_tr_b16 v[162:163], v251 offset:38912\n\t"
    "ds_read_b64_tr_b16 v[164:165], v250 offset:40960\n\t"
    "v_mfma_f32_16x16x32_bf16 v[20:23], v[236:239], v[56:59], v[20:23]\n\t"
    "ds_read_b64_tr_b16 v[166:167], v250 offset:43008\n\t"
    "ds_read_b64_tr_b16 v[168:169], v250 offset:45056\n\t"
    "v_mfma_f32_16x16x32_bf16 v[20:23], v[240:243], v[68:71], v[20:23]\n\t"
    "s_waitcnt lgkmcnt(15)\n\t"
    "v_mfma_f32_16x16x32_bf16 v[0:3], v[176:179], v[72:75], v[0:3]\n\t"
    "ds_read_b64_tr_b16 v[170:171], v250 offset:47104\n\t"
    "v_mfma_f32_16x16x32_bf16 v[0:3], v[244:247], v[84:87], v[0:3]\n\t"
    "ds_read_b64_tr_b16 v[236:237], v251 offset:40960\n\t"
    "ds_read_b64_tr_b16 v[238:239], v251 offset:43008\n\t"
    "v_mfma_f32_16x16x32_bf16 v[8:11], v[176:179], v[76:79], v[8:11]\n\t"
    "ds_read_b64_tr_b16 v[240:241], v251 offset:45056\n\t"
    "ds_read_b64_tr_b16 v[242:243], v251 offset:47104\n\t"
    "v_mfma_f32_16x16x32_bf16 v[8:11], v[244:247], v[88:91], v[8:11]\n\t"
    "v_mfma_f32_16x16x32_bf16 v[16:19], v[176:179], v[80:83], v[16:19]\n\t"
    "v_xor_b32 v248, 0x10000, v248\n\t"
    "v_xor_b32 v249, 0x10000, v249\n\t"
    "v_mfma_f32_16x16x32_bf16 v[16:19], v[244:247], v[92:95], v[16:19]\n\t"
    "s_waitcnt lgkmcnt(15)\n\t"
    "v_xor_b32 v250, 0x10000, v250\n\t"
    "v_mfma_f32_16x16x32_bf16 v[4:7], v[180:183], v[72:75], v[4:7]\n\t"
    "v_xor_b32 v251, 0x10000, v251\n\t"
    "ds_read_b64_tr_b16 v[184:185], v248 offset:0\n\t"
    "v_mfma_f32_16x16x32_bf16 v[4:7], v[172:175], v[84:87], v[4:7]\n\t"
    "ds_read_b64_tr_b16 v[186:187], v248 offset:2048\n\t"
    "ds_read_b64_tr_b16 v[188:189], v248 offset:4096\n\t"
    "v_mfma_f32_16x16x32_bf16 v[12:15], v[180:183], v[76:79], v[12:15]\n\t"
    "ds_read_b64_tr_b16 v[190:191], v248 offset:6144\n\t"
    "ds_read_b64_tr_b16 v[192:193], v249 offset:0\n\t"
    "v_mfma_f32_16x16x32_bf16 v[12:15], v[172:175], v[88:91], v[12:15]\n\t"
    "ds_read_b64_tr_b16 v[194:195], v249 offset:2048\n\t"
    "ds_read_b64_tr_b16 v[196:197], v249 offset:4096\n\t"
    "v_mfma_f32_16x16x32_bf16 v[20:23], v[180:183], v[80:83], v[20:23]\n\t"
    "ds_read_b64_tr_b16 v[198:199], v249 offset:6144\n\t"
    "ds_read_b64_tr_b16 v[200:201], v248 offset:8192\n\t"
    "v_mfma_f32_16x16x32_bf16 v[20:23], v[172:175], v[92:95], v[20:23]\n\t"
    "s_waitcnt lgkmcnt(0)\n\t"
    "s_barrier\n\t"
    "v_mfma_f32_16x16x32_bf16 v[24:27], v[152:155], v[48:51], v[24:27]\n\t"
    "v_cmp_eq_u32 vcc, 0, v252\n\t"
    "v_mov_b32 v148, s66\n\t"
    "v_mfma_f32_16x16x32_bf16 v[24:27], v[232:235], v[60:63], v[24:27]\n\t"
    "v_cvt_pk_bf16_f32 v0, v0, v1\n\t"
    "v_cvt_pk_bf16_f32 v1, v2, v3\n\t"
    "v_mfma_f32_16x16x32_bf16 v[32:35], v[152:155], v[52:55], v[32:35]\n\t"
    "v_cvt_pk_bf16_f32 v2, v4, v5\n\t"
    "v_cvt_pk_bf16_f32 v3, v6, v7\n\t"
    "v_mfma_f32_16x16x32_bf16 v[32:35], v[232:235], v[64:67], v[32:35]\n\t"
    "ds_write_b128 v253, v[0:3] offset:0\n\t"
    "v_mfma_f32_16x16x32_bf16 v[40:43], v[152:155], v[56:59], v[40:43]\n\t"
    "v_cvt_pk_bf16_f32 v8, v8, v9\n\t"
    "v_cvt_pk_bf16_f32 v9, v10, v11\n\t"
    "v_mfma_f32_16x16x32_bf16 v[40:43], v[232:235], v[68:71], v[40:43]\n\t"
    "v_cvt_pk_bf16_f32 v10, v12, v13\n\t"
    "v_mfma_f32_16x16x32_bf16 v[28:31], v[156:159], v[48:51], v[28:31]\n\t"
    "v_cvt_pk_bf16_f32 v11, v14, v15\n\t"
    "v_cvt_pk_bf16_f32 v16, v16, v17\n\t"
    "v_mfma_f32_16x16x32_bf16 v[28:31], v[160:163], v[60:63], v[28:31]\n\t"
    "ds_write_b128 v253, v[8:11] offset:1024\n\t"
    "v_mfma_f32_16x16x32_bf16 v[36:39], v[156:159], v[52:55], v[36:39]\n\t"
    "v_cvt_pk_bf16_f32 v17, v18, v19\n\t"
    "v_cvt_pk_bf16_f32 v18, v20, v21\n\t"
    "v_mfma_f32_16x16x32_bf16 v[36:39], v[160:163], v[64:67], v[36:39]\n\t"
    "v_cvt_pk_bf16_f32 v19, v22, v23\n\t"
    "v_bfrev_b32 v147, -2\n\t"
    "v_mfma_f32_16x16x32_bf16 v[44:47], v[156:159], v[56:59], v[44:47]\n\t"
    "ds_write_b128 v253, v[16:19] offset:2048\n\t"
    "v_mfma_f32_16x16x32_bf16 v[44:47], v[160:163], v[68:71], v[44:47]\n\t"
    "v_mov_b32 v255, s33\n\t"
    "v_mfma_f32_16x16x32_bf16 v[24:27], v[164:167], v[72:75], v[24:27]\n\t"
    "s_waitcnt lgkmcnt(0)\n\t"
    "s_barrier\n\t"
    "v_mfma_f32_16x16x32_bf16 v[24:27], v[168:171], v[84:87], v[24:27]\n\t"
    "v_cndmask_b32 v147, v147, v148, vcc\n\t"
    "v_mov_b32 v149, s32\n\t"
    "v_mfma_f32_16x16x32_bf16 v[32:35], v[164:167], v[76:79], v[32:35]\n\t"
    "ds_read_b64_tr_b16 v[202:203], v248 offset:10240\n\t"
    "ds_read_b64_tr_b16 v[204:205], v248 offset:12288\n\t"
    "v_mfma_f32_16x16x32_bf16 v[32:35], v[168:171], v[88:91], v[32:35]\n\t"
    "ds_read_b64_tr_b16 v[206:207], v248 offset:14336\n\t"
    "ds_read_b64_tr_b16 v[208:209], v249 offset:8192\n\t"
    "v_mfma_f32_16x16x32_bf16 v[40:43], v[164:167], v[80:83], v[40:43]\n\t"
    "ds_read_b64_tr_b16 v[210:211], v249 offset:10240\n\t"
    "ds_read_b64_tr_b16 v[212:213], v249 offset:12288\n\t"
    "v_mfma_f32_16x16x32_bf16 v[40:43], v[168:171], v[92:95], v[40:43]\n\t"
    "ds_read_b64_tr_b16 v[214:215], v249 offset:14336\n\t"
    "v_mfma_f32_16x16x32_bf16 v[28:31], v[236:239], v[72:75], v[28:31]\n\t"
    "ds_read_b64_tr_b16 v[216:217], v250 offset:0\n\t"
    "ds_read_b64_tr_b16 v[218:219], v250 offset:2048\n\t"
    "v_mfma_f32_16x16x32_bf16 v[28:31], v[240:243], v[84:87], v[28:31]\n\t"
    "ds_read_b64_tr_b16 v[220:221], v250 offset:4096\n\t"
    "ds_read_b64_tr_b16 v[222:223], v250 offset:6144\n\t"
    "v_mfma_f32_16x16x32_bf16 v[36:39], v[236:239], v[76:79], v[36:39]\n\t"
    "ds_read_b64_tr_b16 v[224:225], v251 offset:0\n\t"
    "ds_read_b64_tr_b16 v[226:227], v251 offset:2048\n\t"
    "v_mfma_f32_16x16x32_bf16 v[36:39], v[240:243], v[88:91], v[36:39]\n\t"
    "ds_read_b64_tr_b16 v[228:229], v251 offset:4096\n\t"
    "ds_read_b64_tr_b16 v[230:231], v251 offset:6144\n\t"
    "v_mfma_f32_16x16x32_bf16 v[44:47], v[236:239], v[80:83], v[44:47]\n\t"
    "ds_read_b64_tr_b16 v[232:233], v250 offset:8192\n\t"
    "ds_read_b64_tr_b16 v[234:235], v250 offset:10240\n\t"
    "v_mfma_f32_16x16x32_bf16 v[44:47], v[240:243], v[92:95], v[44:47]\n\t"
    "ds_read_b64_tr_b16 v[236:237], v250 offset:12288\n\t"
    "v_mfma_f32_16x16x32_bf16 v[0:3], v[184:187], v[96:99], 0\n\t"
    "ds_read_b64_tr_b16 v[238:239], v250 offset:14336\n\t"
    "ds_read_b64_tr_b16 v[240:241], v251 offset:8192\n\t"
    "v_mfma_f32_16x16x32_bf16 v[0:3], v[188:191], v[108:111], v[0:3]\n\t"
    "v_cvt_pk_bf16_f32 v24, v24, v25\n\t"
    "v_cvt_pk_bf16_f32 v25, v26, v27\n\t"
    "v_mfma_f32_16x16x32_bf16 v[8:11], v[184:187], v[100:103], 0\n\t"
    "v_cvt_pk_bf16_f32 v26, v28, v29\n\t"
    "v_cvt_pk_bf16_f32 v27, v30, v31\n\t"
    "v_mfma_f32_16x16x32_bf16 v[8:11], v[188:191], v[112:115], v[8:11]\n\t"
    "ds_write_b128 v253, v[24:27] offset:12288\n\t"
    "v_mfma_f32_16x16x32_bf16 v[16:19], v[184:187], v[104:107], 0\n\t"
    "v_cvt_pk_bf16_f32 v32, v32, v33\n\t"
    "v_cvt_pk_bf16_f32 v33, v34, v35\n\t"
    "v_mfma_f32_16x16x32_bf16 v[16:19], v[188:191], v[116:119], v[16:19]\n\t"
    "v_cvt_pk_bf16_f32 v34, v36, v37\n\t"
    "v_mfma_f32_16x16x32_bf16 v[4:7], v[192:195], v[96:99], 0\n\t"
    "v_cvt_pk_bf16_f32 v35, v38, v39\n\t"
    "v_cvt_pk_bf16_f32 v40, v40, v41\n\t"
    "v_mfma_f32_16x16x32_bf16 v[4:7], v[196:199], v[108:111], v[4:7]\n\t"
    "ds_write_b128 v253, v[32:35] offset:13312\n\t"
    "v_mfma_f32_16x16x32_bf16 v[12:15], v[192:195], v[100:103], 0\n\t"
    "v_cvt_pk_bf16_f32 v41, v42, v43\n\t"
    "v_cvt_pk_bf16_f32 v42, v44, v45\n\t"
    "v_mfma_f32_16x16x32_bf16 v[12:15], v[196:199], v[112:115], v[12:15]\n\t"
    "ds_read_b64_tr_b16 v[242:243], v251 offset:10240\n\t"
    "ds_read_b64_tr_b16 v[244:245], v251 offset:12288\n\t"
    "ds_read_b64_tr_b16 v[246:247], v251 offset:14336\n\t"
    "v_mfma_f32_16x16x32_bf16 v[20:23], v[192:195], v[104:107], 0\n\t"
    "v_cvt_pk_bf16_f32 v43, v46, v47\n\t"
    "v_mfma_f32_16x16x32_bf16 v[20:23], v[196:199], v[116:119], v[20:23]\n\t"
    "ds_write_b128 v253, v[40:43] offset:14336\n\t"
    "s_waitcnt lgkmcnt(15)\n\t"
    "v_mfma_f32_16x16x32_bf16 v[0:3], v[200:203], v[120:123], v[0:3]\n\t"
    "v_mfma_f32_16x16x32_bf16 v[0:3], v[204:207], v[132:135], v[0:3]\n\t"
    "s_waitcnt lgkmcnt(0)\n\t"
    "s_barrier\n\t"
    "v_mfma_f32_16x16x32_bf16 v[8:11], v[200:203], v[124:127], v[8:11]\n\t"
    "ds_read_b64_tr_b16 v[184:185], v248 offset:32768\n\t"
    "v_mfma_f32_16x16x32_bf16 v[8:11], v[204:207], v[136:139], v[8:11]\n\t"
    "ds_read_b64_tr_b16 v[186:187], v248 offset:34816\n\t"
    "v_mfma_f32_16x16x32_bf16 v[16:19], v[200:203], v[128:131], v[16:19]\n\t"
    "ds_read_b64_tr_b16 v[188:189], v248 offset:36864\n\t"
    "v_mfma_f32_16x16x32_bf16 v[16:19], v[204:207], v[140:143], v[16:19]\n\t"
    "ds_read_b64_tr_b16 v[190:191], v248 offset:38912\n\t"
    "v_mfma_f32_16x16x32_bf16 v[4:7], v[208:211], v[120:123], v[4:7]\n\t"
    "ds_read_b64_tr_b16 v[192:193], v249 offset:32768\n\t"
    "v_mfma_f32_16x16x32_bf16 v[4:7], v[212:215], v[132:135], v[4:7]\n\t"
    "ds_read_b64_tr_b16 v[194:195], v249 offset:34816\n\t"
    "v_mfma_f32_16x16x32_bf16 v[12:15], v[208:211], v[124:127], v[12:15]\n\t"
    "ds_read_b64_tr_b16 v[196:197], v249 offset:36864\n\t"
    "v_mfma_f32_16x16x32_bf16 v[12:15], v[212:215], v[136:139], v[12:15]\n\t"
    "ds_read_b64_tr_b16 v[198:199], v249 offset:38912\n\t"
    "v_mfma_f32_16x16x32_bf16 v[20:23], v[208:211], v[128:131], v[20:23]\n\t"
    "ds_read_b64_tr_b16 v[200:201], v248 offset:40960\n\t"
    "v_mfma_f32_16x16x32_bf16 v[20:23], v[212:215], v[140:143], v[20:23]\n\t"
    "ds_read_b64_tr_b16 v[202:203], v248 offset:43008\n\t"
    "v_mfma_f32_16x16x32_bf16 v[0:3], v[216:219], v[48:51], v[0:3]\n\t"
    "ds_read_b64_tr_b16 v[204:205], v248 offset:45056\n\t"
    "v_mfma_f32_16x16x32_bf16 v[0:3], v[220:223], v[60:63], v[0:3]\n\t"
    "ds_read_b64_tr_b16 v[206:207], v248 offset:47104\n\t"
    "v_mfma_f32_16x16x32_bf16 v[8:11], v[216:219], v[52:55], v[8:11]\n\t"
    "ds_read_b64_tr_b16 v[208:209], v249 offset:40960\n\t"
    "v_mfma_f32_16x16x32_bf16 v[8:11], v[220:223], v[64:67], v[8:11]\n\t"
    "ds_read_b64_tr_b16 v[210:211], v249 offset:43008\n\t"
    "v_mfma_f32_16x16x32_bf16 v[16:19], v[216:219], v[56:59], v[16:19]\n\t"
    "ds_read_b64_tr_b16 v[212:213], v249 offset:45056\n\t"
    "v_mfma_f32_16x16x32_bf16 v[16:19], v[220:223], v[68:71], v[16:19]\n\t"
    "ds_read_b64_tr_b16 v[214:215], v249 offset:47104\n\t"
    "v_mfma_f32_16x16x32_bf16 v[4:7], v[224:227], v[48:51], v[4:7]\n\t"
    "ds_read_b64_tr_b16 v[216:217], v250 offset:32768\n\t"
    "v_mfma_f32_16x16x32_bf16 v[4:7], v[228:231], v[60:63], v[4:7]\n\t"
    "ds_read_b64_tr_b16 v[218:219], v250 offset:34816\n\t"
    "v_mfma_f32_16x16x32_bf16 v[12:15], v[224:227], v[52:55], v[12:15]\n\t"
    "ds_read_b64_tr_b16 v[220:221], v250 offset:36864\n\t"
    "v_mfma_f32_16x16x32_bf16 v[12:15], v[228:231], v[64:67], v[12:15]\n\t"
    "ds_read_b64_tr_b16 v[222:223], v250 offset:38912\n\t"
    "v_mfma_f32_16x16x32_bf16 v[20:23], v[224:227], v[56:59], v[20:23]\n\t"
    "ds_read_b64_tr_b16 v[224:225], v251 offset:32768\n\t"
    "v_mfma_f32_16x16x32_bf16 v[20:23], v[228:231], v[68:71], v[20:23]\n\t"
    "ds_read_b64_tr_b16 v[226:227], v251 offset:34816\n\t"
    "v_mfma_f32_16x16x32_bf16 v[0:3], v[232:235], v[72:75], v[0:3]\n\t"
    "ds_read_b64_tr_b16 v[228:229], v251 offset:36864\n\t"
    "v_mfma_f32_16x16x32_bf16 v[0:3], v[236:239], v[84:87], v[0:3]\n\t"
    "ds_read_b64_tr_b16 v[230:231], v251 offset:38912\n\t"
    "v_mfma_f32_16x16x32_bf16 v[8:11], v[232:235], v[76:79], v[8:11]\n\t"
    "v_mfma_f32_16x16x32_bf16 v[8:11], v[236:239], v[88:91], v[8:11]\n\t"
    "v_mfma_f32_16x16x32_bf16 v[16:19], v[232:235], v[80:83], v[16:19]\n\t"
    "ds_read_b64_tr_b16 v[232:233], v250 offset:40960\n\t"
    "ds_read_b64_tr_b16 v[234:235], v250 offset:43008\n\t"
    "v_mfma_f32_16x16x32_bf16 v[16:19], v[236:239], v[92:95], v[16:19]\n\t"
    "ds_read_b64_tr_b16 v[236:237], v250 offset:45056\n\t"
    "ds_read_b64_tr_b16 v[238:239], v250 offset:47104\n\t"
    "v_mfma_f32_16x16x32_bf16 v[4:7], v[240:243], v[72:75], v[4:7]\n\t"
    "v_mfma_f32_16x16x32_bf16 v[4:7], v[244:247], v[84:87], v[4:7]\n\t"
    "v_mfma_f32_16x16x32_bf16 v[12:15], v[240:243], v[76:79], v[12:15]\n\t"
    "v_mfma_f32_16x16x32_bf16 v[12:15], v[244:247], v[88:91], v[12:15]\n\t"
    "v_mfma_f32_16x16x32_bf16 v[20:23], v[240:243], v[80:83], v[20:23]\n\t"
    "ds_read_b64_tr_b16 v[240:241], v251 offset:40960\n\t"
    "ds_read_b64_tr_b16 v[242:243], v251 offset:43008\n\t"
    "v_mfma_f32_16x16x32_bf16 v[20:23], v[244:247], v[92:95], v[20:23]\n\t"
    "s_waitcnt lgkmcnt(15)\n\t"
    "ds_read_b64_tr_b16 v[244:245], v251 offset:45056\n\t"
    "v_mfma_f32_16x16x32_bf16 v[24:27], v[184:187], v[96:99], 0\n\t"
    "ds_read_b64_tr_b16 v[246:247], v251 offset:47104\n\t"
    "v_mfma_f32_16x16x32_bf16 v[24:27], v[188:191], v[108:111], v[24:27]\n\t"
    "v_cvt_pk_bf16_f32 v0, v0, v1\n\t"
    "v_cvt_pk_bf16_f32 v1, v2, v3\n\t"
    "v_mfma_f32_16x16x32_bf16 v[32:35], v[184:187], v[100:103], 0\n\t"
    "v_cvt_pk_bf16_f32 v2, v4, v5\n\t"
    "v_cvt_pk_bf16_f32 v3, v6, v7\n\t"
    "v_mfma_f32_16x16x32_bf16 v[32:35], v[188:191], v[112:115], v[32:35]\n\t"
    "ds_write_b128 v253, v[0:3] offset:0\n\t"
    "v_mfma_f32_16x16x32_bf16 v[40:43], v[184:187], v[104:107], 0\n\t"
    "v_cvt_pk_bf16_f32 v8, v8, v9\n\t"
    "v_cvt_pk_bf16_f32 v9, v10, v11\n\t"
    "v_mfma_f32_16x16x32_bf16 v[40:43], v[188:191], v[116:119], v[40:43]\n\t"
    "v_cvt_pk_bf16_f32 v10, v12, v13\n\t"
    "v_mfma_f32_16x16x32_bf16 v[28:31], v[192:195], v[96:99], 0\n\t"
    "v_cvt_pk_bf16_f32 v11, v14, v15\n\t"
    "v_cvt_pk_bf16_f32 v16, v16, v17\n\t"
    "v_mfma_f32_16x16x32_bf16 v[28:31], v[196:199], v[108:111], v[28:31]\n\t"
    "ds_write_b128 v253, v[8:11] offset:1024\n\t"
    "v_mfma_f32_16x16x32_bf16 v[36:39], v[192:195], v[100:103], 0\n\t"
    "v_cvt_pk_bf16_f32 v17, v18, v19\n\t"
    "v_cvt_pk_bf16_f32 v18, v20, v21\n\t"
    "v_mfma_f32_16x16x32_bf16 v[36:39], v[196:199], v[112:115], v[36:39]\n\t"
    "v_cvt_pk_bf16_f32 v19, v22, v23\n\t"
    "v_mfma_f32_16x16x32_bf16 v[44:47], v[192:195], v[104:107], 0\n\t"
    "ds_write_b128 v253, v[16:19] offset:2048\n\t"
    "v_mfma_f32_16x16x32_bf16 v[44:47], v[196:199], v[116:119], v[44:47]\n\t"
    "v_mfma_f32_16x16x32_bf16 v[24:27], v[200:203], v[120:123], v[24:27]\n\t"
    "v_mfma_f32_16x16x32_bf16 v[24:27], v[204:207], v[132:135], v[24:27]\n\t"
    "s_waitcnt lgkmcnt(0)\n\t"
    "s_barrier\n\t"
    "v_mfma_f32_16x16x32_bf16 v[32:35], v[200:203], v[124:127], v[32:35]\n\t"
    "v_xor_b32 v248, 0x10000, v248\n\t"
    "v_xor_b32 v249, 0x10000, v249\n\t"
    "v_mfma_f32_16x16x32_bf16 v[32:35], v[204:207], v[136:139], v[32:35]\n\t"
    "v_xor_b32 v250, 0x10000, v250\n\t"
    "v_xor_b32 v251, 0x10000, v251\n\t"
    "v_mfma_f32_16x16x32_bf16 v[40:43], v[200:203], v[128:131], v[40:43]\n\t"
    "ds_read_b64_tr_b16 v[184:185], v248 offset:0\n\t"
    "ds_read_b64_tr_b16 v[186:187], v248 offset:2048\n\t"
    "v_mfma_f32_16x16x32_bf16 v[40:43], v[204:207], v[140:143], v[40:43]\n\t"
    "ds_read_b64_tr_b16 v[188:189], v248 offset:4096\n\t"
    "ds_read_b64_tr_b16 v[190:191], v248 offset:6144\n\t"
    "v_mfma_f32_16x16x32_bf16 v[28:31], v[208:211], v[120:123], v[28:31]\n\t"
    "ds_read_b64_tr_b16 v[192:193], v249 offset:0\n\t"
    "v_mfma_f32_16x16x32_bf16 v[28:31], v[212:215], v[132:135], v[28:31]\n\t"
    "ds_read_b64_tr_b16 v[194:195], v249 offset:2048\n\t"
    "v_mfma_f32_16x16x32_bf16 v[36:39], v[208:211], v[124:127], v[36:39]\n\t"
    "ds_read_b64_tr_b16 v[196:197], v249 offset:4096\n\t"
    "v_mfma_f32_16x16x32_bf16 v[36:39], v[212:215], v[136:139], v[36:39]\n\t"
    "ds_read_b64_tr_b16 v[198:199], v249 offset:6144\n\t"
    "v_mfma_f32_16x16x32_bf16 v[44:47], v[208:211], v[128:131], v[44:47]\n\t"
    "ds_read_b64_tr_b16 v[200:201], v248 offset:8192\n\t"
    "v_mfma_f32_16x16x32_bf16 v[44:47], v[212:215], v[140:143], v[44:47]\n\t"
    "ds_read_b64_tr_b16 v[202:203], v248 offset:10240\n\t"
    "v_mfma_f32_16x16x32_bf16 v[24:27], v[216:219], v[48:51], v[24:27]\n\t"
    "ds_read_b64_tr_b16 v[204:205], v248 offset:12288\n\t"
    "v_mfma_f32_16x16x32_bf16 v[24:27], v[220:223], v[60:63], v[24:27]\n\t"
    "ds_read_b64_tr_b16 v[206:207], v248 offset:14336\n\t"
    "v_mfma_f32_16x16x32_bf16 v[32:35], v[216:219], v[52:55], v[32:35]\n\t"
    "ds_read_b64_tr_b16 v[208:209], v249 offset:8192\n\t"
    "v_mfma_f32_16x16x32_bf16 v[32:35], v[220:223], v[64:67], v[32:35]\n\t"
    "ds_read_b64_tr_b16 v[210:211], v249 offset:10240\n\t"
    "v_mfma_f32_16x16x32_bf16 v[40:43], v[216:219], v[56:59], v[40:43]\n\t"
    "ds_read_b64_tr_b16 v[212:213], v249 offset:12288\n\t"
    "v_mfma_f32_16x16x32_bf16 v[40:43], v[220:223], v[68:71], v[40:43]\n\t"
    "ds_read_b64_tr_b16 v[214:215], v249 offset:14336\n\t"
    "v_mfma_f32_16x16x32_bf16 v[28:31], v[224:227], v[48:51], v[28:31]\n\t"
    "ds_read_b64_tr_b16 v[216:217], v250 offset:0\n\t"
    "v_mfma_f32_16x16x32_bf16 v[28:31], v[228:231], v[60:63], v[28:31]\n\t"
    "ds_read_b64_tr_b16 v[218:219], v250 offset:2048\n\t"
    "v_mfma_f32_16x16x32_bf16 v[36:39], v[224:227], v[52:55], v[36:39]\n\t"
    "ds_read_b64_tr_b16 v[220:221], v250 offset:4096\n\t"
    "v_mfma_f32_16x16x32_bf16 v[36:39], v[228:231], v[64:67], v[36:39]\n\t"
    "ds_read_b64_tr_b16 v[222:223], v250 offset:6144\n\t"
    "v_mfma_f32_16x16x32_bf16 v[44:47], v[224:227], v[56:59], v[44:47]\n\t"
    "ds_read_b64_tr_b16 v[224:225], v251 offset:0\n\t"
    "v_mfma_f32_16x16x32_bf16 v[44:47], v[228:231], v[68:71], v[44:47]\n\t"
    "ds_read_b64_tr_b16 v[226:227], v251 offset:2048\n\t"
    "v_mfma_f32_16x16x32_bf16 v[24:27], v[232:235], v[72:75], v[24:27]\n\t"
    "ds_read_b64_tr_b16 v[228:229], v251 offset:4096\n\t"
    "v_mfma_f32_16x16x32_bf16 v[24:27], v[236:239], v[84:87], v[24:27]\n\t"
    "ds_read_b64_tr_b16 v[230:231], v251 offset:6144\n\t"
    "v_mfma_f32_16x16x32_bf16 v[32:35], v[232:235], v[76:79], v[32:35]\n\t"
    "v_mfma_f32_16x16x32_bf16 v[32:35], v[236:239], v[88:91], v[32:35]\n\t"
    "v_mfma_f32_16x16x32_bf16 v[40:43], v[232:235], v[80:83], v[40:43]\n\t"
    "ds_read_b64_tr_b16 v[232:233], v250 offset:8192\n\t"
    "ds_read_b64_tr_b16 v[234:235], v250 offset:10240\n\t"
    "v_mfma_f32_16x16x32_bf16 v[40:43], v[236:239], v[92:95], v[40:43]\n\t"
    "ds_read_b64_tr_b16 v[236:237], v250 offset:12288\n\t"
    "ds_read_b64_tr_b16 v[238:239], v250 offset:14336\n\t"
    "v_mfma_f32_16x16x32_bf16 v[28:31], v[240:243], v[72:75], v[28:31]\n\t"
    "v_mfma_f32_16x16x32_bf16 v[28:31], v[244:247], v[84:87], v[28:31]\n\t"
    "v_mfma_f32_16x16x32_bf16 v[36:39], v[240:243], v[76:79], v[36:39]\n\t"
    "v_mfma_f32_16x16x32_bf16 v[36:39], v[244:247], v[88:91], v[36:39]\n\t"
    "v_mfma_f32_16x16x32_bf16 v[44:47], v[240:243], v[80:83], v[44:47]\n\t"
    "ds_read_b64_tr_b16 v[240:241], v251 offset:8192\n\t"
    "ds_read_b64_tr_b16 v[242:243], v251 offset:10240\n\t"
    "v_mfma_f32_16x16x32_bf16 v[44:47], v[244:247], v[92:95], v[44:47]\n\t"
    "s_mov_b32 s8, 1\n\t"
    "gqa_p3_0:\n\t"
    "s_waitcnt lgkmcnt(15)\n\t"
    "ds_read_b64_tr_b16 v[244:245], v251 offset:12288\n\t"
    "v_mfma_f32_16x16x32_bf16 v[0:3], v[184:187], v[96:99], 0\n\t"
    "ds_read_b64_tr_b16 v[246:247], v251 offset:14336\n\t"
    "v_mfma_f32_16x16x32_bf16 v[0:3], v[188:191], v[108:111], v[0:3]\n\t"
    "v_cvt_pk_bf16_f32 v24, v24, v25\n\t"
    "v_cvt_pk_bf16_f32 v25, v26, v27\n\t"
    "v_mfma_f32_16x16x32_bf16 v[8:11], v[184:187], v[100:103], 0\n\t"
    "v_cvt_pk_bf16_f32 v26, v28, v29\n\t"
    "v_cvt_pk_bf16_f32 v27, v30, v31\n\t"
    "v_mfma_f32_16x16x32_bf16 v[8:11], v[188:191], v[112:115], v[8:11]\n\t"
    "ds_write_b128 v253, v[24:27] offset:12288\n\t"
    "v_mfma_f32_16x16x32_bf16 v[16:19], v[184:187], v[104:107], 0\n\t"
    "v_cvt_pk_bf16_f32 v32, v32, v33\n\t"
    "v_cvt_pk_bf16_f32 v33, v34, v35\n\t"
    "v_mfma_f32_16x16x32_bf16 v[16:19], v[188:191], v[116:119], v[16:19]\n\t"
    "v_cvt_pk_bf16_f32 v34, v36, v37\n\t"
    "v_mfma_f32_16x16x32_bf16 v[4:7], v[192:195], v[96:99], 0\n\t"
    "v_cvt_pk_bf16_f32 v35, v38, v39\n\t"
    "v_cvt_pk_bf16_f32 v40, v40, v41\n\t"
    "v_mfma_f32_16x16x32_bf16 v[4:7], v[196:199], v[108:111], v[4:7]\n\t"
    "ds_write_b128 v253, v[32:35] offset:13312\n\t"
    "v_mfma_f32_16x16x32_bf16 v[12:15], v[192:195], v[100:103], 0\n\t"
    "v_cvt_pk_bf16_f32 v41, v42, v43\n\t"
    "v_cvt_pk_bf16_f32 v42, v44, v45\n\t"
    "v_mfma_f32_16x16x32_bf16 v[12:15], v[196:199], v[112:115], v[12:15]\n\t"
    "v_cvt_pk_bf16_f32 v43, v46, v47\n\t"
    "v_mfma_f32_16x16x32_bf16 v[20:23], v[192:195], v[104:107], 0\n\t"
    "ds_write_b128 v253, v[40:43] offset:14336\n\t"
    "v_mfma_f32_16x16x32_bf16 v[20:23], v[196:199], v[116:119], v[20:23]\n\t"
    "v_mfma_f32_16x16x32_bf16 v[0:3], v[200:203], v[120:123], v[0:3]\n\t"
    "v_mfma_f32_16x16x32_bf16 v[0:3], v[204:207], v[132:135], v[0:3]\n\t"
    "s_waitcnt lgkmcnt(0)\n\t"
    "s_barrier\n\t"
    "v_mfma_f32_16x16x32_bf16 v[8:11], v[200:203], v[124:127], v[8:11]\n\t"
    "ds_read_b64_tr_b16 v[184:185], v248 offset:32768\n\t"
    "v_mfma_f32_16x16x32_bf16 v[8:11], v[204:207], v[136:139], v[8:11]\n\t"
    "ds_read_b64_tr_b16 v[186:187], v248 offset:34816\n\t"
    "v_mfma_f32_16x16x32_bf16 v[16:19], v[200:203], v[128:131], v[16:19]\n\t"
    "ds_read_b64_tr_b16 v[188:189], v248 offset:36864\n\t"
    "v_mfma_f32_16x16x32_bf16 v[16:19], v[204:207], v[140:143], v[16:19]\n\t"
    "ds_read_b64_tr_b16 v[190:191], v248 offset:38912\n\t"
    "v_mfma_f32_16x16x32_bf16 v[4:7], v[208:211], v[120:123], v[4:7]\n\t"
    "ds_read_b64_tr_b16 v[192:193], v249 offset:32768\n\t"
    "v_mfma_f32_16x16x32_bf16 v[4:7], v[212:215], v[132:135], v[4:7]\n\t"
    "ds_read_b64_tr_b16 v[194:195], v249 offset:34816\n\t"
    "v_mfma_f32_16x16x32_bf16 v[12:15], v[208:211], v[124:127], v[12:15]\n\t"
    "ds_read_b64_tr_b16 v[196:197], v249 offset:36864\n\t"
    "v_mfma_f32_16x16x32_bf16 v[12:15], v[212:215], v[136:139], v[12:15]\n\t"
    "ds_read_b64_tr_b16 v[198:199], v249 offset:38912\n\t"
    "v_mfma_f32_16x16x32_bf16 v[20:23], v[208:211], v[128:131], v[20:23]\n\t"
    "ds_read_b64_tr_b16 v[200:201], v248 offset:40960\n\t"
    "v_mfma_f32_16x16x32_bf16 v[20:23], v[212:215], v[140:143], v[20:23]\n\t"
    "ds_read_b64_tr_b16 v[202:203], v248 offset:43008\n\t"
    "v_mfma_f32_16x16x32_bf16 v[0:3], v[216:219], v[48:51], v[0:3]\n\t"
    "ds_read_b64_tr_b16 v[204:205], v248 offset:45056\n\t"
    "v_mfma_f32_16x16x32_bf16 v[0:3], v[220:223], v[60:63], v[0:3]\n\t"
    "ds_read_b64_tr_b16 v[206:207], v248 offset:47104\n\t"
    "v_mfma_f32_16x16x32_bf16 v[8:11], v[216:219], v[52:55], v[8:11]\n\t"
    "ds_read_b64_tr_b16 v[208:209], v249 offset:40960\n\t"
    "v_mfma_f32_16x16x32_bf16 v[8:11], v[220:223], v[64:67], v[8:11]\n\t"
    "ds_read_b64_tr_b16 v[210:211], v249 offset:43008\n\t"
    "v_mfma_f32_16x16x32_bf16 v[16:19], v[216:219], v[56:59], v[16:19]\n\t"
    "ds_read_b64_tr_b16 v[212:213], v249 offset:45056\n\t"
    "v_mfma_f32_16x16x32_bf16 v[16:19], v[220:223], v[68:71], v[16:19]\n\t"
    "ds_read_b64_tr_b16 v[214:215], v249 offset:47104\n\t"
    "v_mfma_f32_16x16x32_bf16 v[4:7], v[224:227], v[48:51], v[4:7]\n\t"
    "ds_read_b64_tr_b16 v[216:217], v250 offset:32768\n\t"
    "v_mfma_f32_16x16x32_bf16 v[4:7], v[228:231], v[60:63], v[4:7]\n\t"
    "ds_read_b64_tr_b16 v[218:219], v250 offset:34816\n\t"
    "v_mfma_f32_16x16x32_bf16 v[12:15], v[224:227], v[52:55], v[12:15]\n\t"
    "ds_read_b64_tr_b16 v[220:221], v250 offset:36864\n\t"
    "v_mfma_f32_16x16x32_bf16 v[12:15], v[228:231], v[64:67], v[12:15]\n\t"
    "ds_read_b64_tr_b16 v[222:223], v250 offset:38912\n\t"
    "v_mfma_f32_16x16x32_bf16 v[20:23], v[224:227], v[56:59], v[20:23]\n\t"
    "ds_read_b64_tr_b16 v[224:225], v251 offset:32768\n\t"
    "v_mfma_f32_16x16x32_bf16 v[20:23], v[228:231], v[68:71], v[20:23]\n\t"
    "ds_read_b64_tr_b16 v[226:227], v251 offset:34816\n\t"
    "v_mfma_f32_16x16x32_bf16 v[0:3], v[232:235], v[72:75], v[0:3]\n\t"
    "ds_read_b64_tr_b16 v[228:229], v251 offset:36864\n\t"
    "v_mfma_f32_16x16x32_bf16 v[0:3], v[236:239], v[84:87], v[0:3]\n\t"
    "ds_read_b64_tr_b16 v[230:231], v251 offset:38912\n\t"
    "v_mfma_f32_16x16x32_bf16 v[8:11], v[232:235], v[76:79], v[8:11]\n\t"
    "v_mfma_f32_16x16x32_bf16 v[8:11], v[236:239], v[88:91], v[8:11]\n\t"
    "v_mfma_f32_16x16x32_bf16 v[16:19], v[232:235], v[80:83], v[16:19]\n\t"
    "ds_read_b64_tr_b16 v[232:233], v250 offset:40960\n\t"
    "ds_read_b64_tr_b16 v[234:235], v250 offset:43008\n\t"
    "v_mfma_f32_16x16x32_bf16 v[16:19], v[236:239], v[92:95], v[16:19]\n\t"
    "ds_read_b64_tr_b16 v[236:237], v250 offset:45056\n\t"
    "ds_read_b64_tr_b16 v[238:239], v250 offset:47104\n\t"
    "v_mfma_f32_16x16x32_bf16 v[4:7], v[240:243], v[72:75], v[4:7]\n\t"
    "v_mfma_f32_16x16x32_bf16 v[4:7], v[244:247], v[84:87], v[4:7]\n\t"
    "v_mfma_f32_16x16x32_bf16 v[12:15], v[240:243], v[76:79], v[12:15]\n\t"
    "v_mfma_f32_16x16x32_bf16 v[12:15], v[244:247], v[88:91], v[12:15]\n\t"
    "v_mfma_f32_16x16x32_bf16 v[20:23], v[240:243], v[80:83], v[20:23]\n\t"
    "ds_read_b64_tr_b16 v[240:241], v251 offset:40960\n\t"
    "ds_read_b64_tr_b16 v[242:243], v251 offset:43008\n\t"
    "v_mfma_f32_16x16x32_bf16 v[20:23], v[244:247], v[92:95], v[20:23]\n\t"
    "s_waitcnt lgkmcnt(15)\n\t"
    "ds_read_b64_tr_b16 v[244:245], v251 offset:45056\n\t"
    "v_mfma_f32_16x16x32_bf16 v[24:27], v[184:187], v[96:99], 0\n\t"
    "ds_read_b64_tr_b16 v[246:247], v251 offset:47104\n\t"
    "v_mfma_f32_16x16x32_bf16 v[24:27], v[188:191], v[108:111], v[24:27]\n\t"
    "v_cvt_pk_bf16_f32 v0, v0, v1\n\t"
    "v_cvt_pk_bf16_f32 v1, v2, v3\n\t"
    "v_mfma_f32_16x16x32_bf16 v[32:35], v[184:187], v[100:103], 0\n\t"
    "v_cvt_pk_bf16_f32 v2, v4, v5\n\t"
    "v_cvt_pk_bf16_f32 v3, v6, v7\n\t"
    "v_mfma_f32_16x16x32_bf16 v[32:35], v[188:191], v[112:115], v[32:35]\n\t"
    "ds_write_b128 v253, v[0:3] offset:0\n\t"
    "v_mfma_f32_16x16x32_bf16 v[40:43], v[184:187], v[104:107], 0\n\t"
    "v_cvt_pk_bf16_f32 v8, v8, v9\n\t"
    "v_cvt_pk_bf16_f32 v9, v10, v11\n\t"
    "v_mfma_f32_16x16x32_bf16 v[40:43], v[188:191], v[116:119], v[40:43]\n\t"
    "v_cvt_pk_bf16_f32 v10, v12, v13\n\t"
    "v_mfma_f32_16x16x32_bf16 v[28:31], v[192:195], v[96:99], 0\n\t"
    "v_cvt_pk_bf16_f32 v11, v14, v15\n\t"
    "v_cvt_pk_bf16_f32 v16, v16, v17\n\t"
    "v_mfma_f32_16x16x32_bf16 v[28:31], v[196:199], v[108:111], v[28:31]\n\t"
    "ds_write_b128 v253, v[8:11] offset:1024\n\t"
    "v_mfma_f32_16x16x32_bf16 v[36:39], v[192:195], v[100:103], 0\n\t"
    "v_cvt_pk_bf16_f32 v17, v18, v19\n\t"
    "v_cvt_pk_bf16_f32 v18, v20, v21\n\t"
    "v_mfma_f32_16x16x32_bf16 v[36:39], v[196:199], v[112:115], v[36:39]\n\t"
    "v_cvt_pk_bf16_f32 v19, v22, v23\n\t"
    "v_mfma_f32_16x16x32_bf16 v[44:47], v[192:195], v[104:107], 0\n\t"
    "ds_write_b128 v253, v[16:19] offset:2048\n\t"
    "v_mfma_f32_16x16x32_bf16 v[44:47], v[196:199], v[116:119], v[44:47]\n\t"
    "v_mfma_f32_16x16x32_bf16 v[24:27], v[200:203], v[120:123], v[24:27]\n\t"
    "v_mfma_f32_16x16x32_bf16 v[24:27], v[204:207], v[132:135], v[24:27]\n\t"
    "s_waitcnt lgkmcnt(0)\n\t"
    "s_barrier\n\t"
    "v_mfma_f32_16x16x32_bf16 v[32:35], v[200:203], v[124:127], v[32:35]\n\t"
    "v_xor_b32 v248, 0x10000, v248\n\t"
    "v_xor_b32 v249, 0x10000, v249\n\t"
    "v_mfma_f32_16x16x32_bf16 v[32:35], v[204:207], v[136:139], v[32:35]\n\t"
    "v_xor_b32 v250, 0x10000, v250\n\t"
    "v_xor_b32 v251, 0x10000, v251\n\t"
    "v_mfma_f32_16x16x32_bf16 v[40:43], v[200:203], v[128:131], v[40:43]\n\t"
    "ds_read_b64_tr_b16 v[184:185], v248 offset:0\n\t"
    "ds_read_b64_tr_b16 v[186:187], v248 offset:2048\n\t"
    "v_mfma_f32_16x16x32_bf16 v[40:43], v[204:207], v[140:143], v[40:43]\n\t"
    "ds_read_b64_tr_b16 v[188:189], v248 offset:4096\n\t"
    "ds_read_b64_tr_b16 v[190:191], v248 offset:6144\n\t"
    "v_mfma_f32_16x16x32_bf16 v[28:31], v[208:211], v[120:123], v[28:31]\n\t"
    "ds_read_b64_tr_b16 v[192:193], v249 offset:0\n\t"
    "v_mfma_f32_16x16x32_bf16 v[28:31], v[212:215], v[132:135], v[28:31]\n\t"
    "ds_read_b64_tr_b16 v[194:195], v249 offset:2048\n\t"
    "v_mfma_f32_16x16x32_bf16 v[36:39], v[208:211], v[124:127], v[36:39]\n\t"
    "ds_read_b64_tr_b16 v[196:197], v249 offset:4096\n\t"
    "v_mfma_f32_16x16x32_bf16 v[36:39], v[212:215], v[136:139], v[36:39]\n\t"
    "ds_read_b64_tr_b16 v[198:199], v249 offset:6144\n\t"
    "v_mfma_f32_16x16x32_bf16 v[44:47], v[208:211], v[128:131], v[44:47]\n\t"
    "ds_read_b64_tr_b16 v[200:201], v248 offset:8192\n\t"
    "v_mfma_f32_16x16x32_bf16 v[44:47], v[212:215], v[140:143], v[44:47]\n\t"
    "ds_read_b64_tr_b16 v[202:203], v248 offset:10240\n\t"
    "v_mfma_f32_16x16x32_bf16 v[24:27], v[216:219], v[48:51], v[24:27]\n\t"
    "ds_read_b64_tr_b16 v[204:205], v248 offset:12288\n\t"
    "v_mfma_f32_16x16x32_bf16 v[24:27], v[220:223], v[60:63], v[24:27]\n\t"
    "ds_read_b64_tr_b16 v[206:207], v248 offset:14336\n\t"
    "v_mfma_f32_16x16x32_bf16 v[32:35], v[216:219], v[52:55], v[32:35]\n\t"
    "ds_read_b64_tr_b16 v[208:209], v249 offset:8192\n\t"
    "v_mfma_f32_16x16x32_bf16 v[32:35], v[220:223], v[64:67], v[32:35]\n\t"
    "ds_read_b64_tr_b16 v[210:211], v249 offset:10240\n\t"
    "v_mfma_f32_16x16x32_bf16 v[40:43], v[216:219], v[56:59], v[40:43]\n\t"
    "ds_read_b64_tr_b16 v[212:213], v249 offset:12288\n\t"
    "v_mfma_f32_16x16x32_bf16 v[40:43], v[220:223], v[68:71], v[40:43]\n\t"
    "ds_read_b64_tr_b16 v[214:215], v249 offset:14336\n\t"
    "v_mfma_f32_16x16x32_bf16 v[28:31], v[224:227], v[48:51], v[28:31]\n\t"
    "ds_read_b64_tr_b16 v[216:217], v250 offset:0\n\t"
    "v_mfma_f32_16x16x32_bf16 v[28:31], v[228:231], v[60:63], v[28:31]\n\t"
    "ds_read_b64_tr_b16 v[218:219], v250 offset:2048\n\t"
    "v_mfma_f32_16x16x32_bf16 v[36:39], v[224:227], v[52:55], v[36:39]\n\t"
    "ds_read_b64_tr_b16 v[220:221], v250 offset:4096\n\t"
    "v_mfma_f32_16x16x32_bf16 v[36:39], v[228:231], v[64:67], v[36:39]\n\t"
    "ds_read_b64_tr_b16 v[222:223], v250 offset:6144\n\t"
    "v_mfma_f32_16x16x32_bf16 v[44:47], v[224:227], v[56:59], v[44:47]\n\t"
    "ds_read_b64_tr_b16 v[224:225], v251 offset:0\n\t"
    "v_mfma_f32_16x16x32_bf16 v[44:47], v[228:231], v[68:71], v[44:47]\n\t"
    "ds_read_b64_tr_b16 v[226:227], v251 offset:2048\n\t"
    "v_mfma_f32_16x16x32_bf16 v[24:27], v[232:235], v[72:75], v[24:27]\n\t"
    "ds_read_b64_tr_b16 v[228:229], v251 offset:4096\n\t"
    "v_mfma_f32_16x16x32_bf16 v[24:27], v[236:239], v[84:87], v[24:27]\n\t"
    "ds_read_b64_tr_b16 v[230:231], v251 offset:6144\n\t"
    "v_mfma_f32_16x16x32_bf16 v[32:35], v[232:235], v[76:79], v[32:35]\n\t"
    "v_mfma_f32_16x16x32_bf16 v[32:35], v[236:239], v[88:91], v[32:35]\n\t"
    "v_mfma_f32_16x16x32_bf16 v[40:43], v[232:235], v[80:83], v[40:43]\n\t"
    "ds_read_b64_tr_b16 v[232:233], v250 offset:8192\n\t"
    "ds_read_b64_tr_b16 v[234:235], v250 offset:10240\n\t"
    "v_mfma_f32_16x16x32_bf16 v[40:43], v[236:239], v[92:95], v[40:43]\n\t"
    "ds_read_b64_tr_b16 v[236:237], v250 offset:12288\n\t"
    "ds_read_b64_tr_b16 v[238:239], v250 offset:14336\n\t"
    "v_mfma_f32_16x16x32_bf16 v[28:31], v[240:243], v[72:75], v[28:31]\n\t"
    "v_mfma_f32_16x16x32_bf16 v[28:31], v[244:247], v[84:87], v[28:31]\n\t"
    "v_mfma_f32_16x16x32_bf16 v[36:39], v[240:243], v[76:79], v[36:39]\n\t"
    "v_mfma_f32_16x16x32_bf16 v[36:39], v[244:247], v[88:91], v[36:39]\n\t"
    "v_mfma_f32_16x16x32_bf16 v[44:47], v[240:243], v[80:83], v[44:47]\n\t"
    "ds_read_b64_tr_b16 v[240:241], v251 offset:8192\n\t"
    "ds_read_b64_tr_b16 v[242:243], v251 offset:10240\n\t"
    "v_mfma_f32_16x16x32_bf16 v[44:47], v[244:247], v[92:95], v[44:47]\n\t"
    "s_sub_u32 s8, s8, 1\n\t"
    "s_cbranch_scc0 gqa_p3_0\n\t"
    "s_waitcnt lgkmcnt(15)\n\t"
    "ds_read_b64_tr_b16 v[244:245], v251 offset:12288\n\t"
    "v_mfma_f32_16x16x32_bf16 v[0:3], v[184:187], v[96:99], 0\n\t"
    "ds_read_b64_tr_b16 v[246:247], v251 offset:14336\n\t"
    "v_mfma_f32_16x16x32_bf16 v[0:3], v[188:191], v[108:111], v[0:3]\n\t"
    "v_cvt_pk_bf16_f32 v24, v24, v25\n\t"
    "v_cvt_pk_bf16_f32 v25, v26, v27\n\t"
    "v_mfma_f32_16x16x32_bf16 v[8:11], v[184:187], v[100:103], 0\n\t"
    "v_cvt_pk_bf16_f32 v26, v28, v29\n\t"
    "v_cvt_pk_bf16_f32 v27, v30, v31\n\t"
    "v_mfma_f32_16x16x32_bf16 v[8:11], v[188:191], v[112:115], v[8:11]\n\t"
    "ds_write_b128 v253, v[24:27] offset:12288\n\t"
    "v_mfma_f32_16x16x32_bf16 v[16:19], v[184:187], v[104:107], 0\n\t"
    "v_cvt_pk_bf16_f32 v32, v32, v33\n\t"
    "v_cvt_pk_bf16_f32 v33, v34, v35\n\t"
    "v_mfma_f32_16x16x32_bf16 v[16:19], v[188:191], v[116:119], v[16:19]\n\t"
    "v_cvt_pk_bf16_f32 v34, v36, v37\n\t"
    "v_mfma_f32_16x16x32_bf16 v[4:7], v[192:195], v[96:99], 0\n\t"
    "v_cvt_pk_bf16_f32 v35, v38, v39\n\t"
    "v_cvt_pk_bf16_f32 v40, v40, v41\n\t"
    "v_mfma_f32_16x16x32_bf16 v[4:7], v[196:199], v[108:111], v[4:7]\n\t"
    "ds_write_b128 v253, v[32:35] offset:13312\n\t"
    "v_mfma_f32_16x16x32_bf16 v[12:15], v[192:195], v[100:103], 0\n\t"
    "v_cvt_pk_bf16_f32 v41, v42, v43\n\t"
    "v_cvt_pk_bf16_f32 v42, v44, v45\n\t"
    "v_mfma_f32_16x16x32_bf16 v[12:15], v[196:199], v[112:115], v[12:15]\n\t"
    "v_cvt_pk_bf16_f32 v43, v46, v47\n\t"
    "v_mfma_f32_16x16x32_bf16 v[20:23], v[192:195], v[104:107], 0\n\t"
    "ds_write_b128 v253, v[40:43] offset:14336\n\t"
    "v_mfma_f32_16x16x32_bf16 v[20:23], v[196:199], v[116:119], v[20:23]\n\t"
    "v_mfma_f32_16x16x32_bf16 v[0:3], v[200:203], v[120:123], v[0:3]\n\t"
    "v_mfma_f32_16x16x32_bf16 v[0:3], v[204:207], v[132:135], v[0:3]\n\t"
    "s_waitcnt lgkmcnt(0)\n\t"
    "s_barrier\n\t"
    "v_mfma_f32_16x16x32_bf16 v[8:11], v[200:203], v[124:127], v[8:11]\n\t"
    "v_mfma_f32_16x16x32_bf16 v[8:11], v[204:207], v[136:139], v[8:11]\n\t"
    "v_mfma_f32_16x16x32_bf16 v[16:19], v[200:203], v[128:131], v[16:19]\n\t"
    "v_mfma_f32_16x16x32_bf16 v[16:19], v[204:207], v[140:143], v[16:19]\n\t"
    "v_mfma_f32_16x16x32_bf16 v[4:7], v[208:211], v[120:123], v[4:7]\n\t"
    "v_mfma_f32_16x16x32_bf16 v[4:7], v[212:215], v[132:135], v[4:7]\n\t"
    "v_mfma_f32_16x16x32_bf16 v[12:15], v[208:211], v[124:127], v[12:15]\n\t"
    "v_mfma_f32_16x16x32_bf16 v[12:15], v[212:215], v[136:139], v[12:15]\n\t"
    "v_mfma_f32_16x16x32_bf16 v[20:23], v[208:211], v[128:131], v[20:23]\n\t"
    "v_mfma_f32_16x16x32_bf16 v[20:23], v[212:215], v[140:143], v[20:23]\n\t"
    "v_mfma_f32_16x16x32_bf16 v[0:3], v[216:219], v[48:51], v[0:3]\n\t"
    "v_mfma_f32_16x16x32_bf16 v[0:3], v[220:223], v[60:63], v[0:3]\n\t"
    "v_mfma_f32_16x16x32_bf16 v[8:11], v[216:219], v[52:55], v[8:11]\n\t"
    "v_mfma_f32_16x16x32_bf16 v[8:11], v[220:223], v[64:67], v[8:11]\n\t"
    "v_mfma_f32_16x16x32_bf16 v[16:19], v[216:219], v[56:59], v[16:19]\n\t"
    "v_mfma_f32_16x16x32_bf16 v[16:19], v[220:223], v[68:71], v[16:19]\n\t"
    "v_mfma_f32_16x16x32_bf16 v[4:7], v[224:227], v[48:51], v[4:7]\n\t"
    "v_mfma_f32_16x16x32_bf16 v[4:7], v[228:231], v[60:63], v[4:7]\n\t"
    "v_mfma_f32_16x16x32_bf16 v[12:15], v[224:227], v[52:55], v[12:15]\n\t"
    "v_mfma_f32_16x16x32_bf16 v[12:15], v[228:231], v[64:67], v[12:15]\n\t"
    "v_mfma_f32_16x16x32_bf16 v[20:23], v[224:227], v[56:59], v[20:23]\n\t"
    "v_mfma_f32_16x16x32_bf16 v[20:23], v[228:231], v[68:71], v[20:23]\n\t"
    "v_mfma_f32_16x16x32_bf16 v[0:3], v[232:235], v[72:75], v[0:3]\n\t"
    "v_mfma_f32_16x16x32_bf16 v[0:3], v[236:239], v[84:87], v[0:3]\n\t"
    "v_mfma_f32_16x16x32_bf16 v[8:11], v[232:235], v[76:79], v[8:11]\n\t"
    "v_mfma_f32_16x16x32_bf16 v[8:11], v[236:239], v[88:91], v[8:11]\n\t"
    "v_mfma_f32_16x16x32_bf16 v[16:19], v[232:235], v[80:83], v[16:19]\n\t"
    "v_mfma_f32_16x16x32_bf16 v[16:19], v[236:239], v[92:95], v[16:19]\n\t"
    "v_mfma_f32_16x16x32_bf16 v[4:7], v[240:243], v[72:75], v[4:7]\n\t"
    "v_mfma_f32_16x16x32_bf16 v[4:7], v[244:247], v[84:87], v[4:7]\n\t"
    "v_mfma_f32_16x16x32_bf16 v[12:15], v[240:243], v[76:79], v[12:15]\n\t"
    "v_mfma_f32_16x16x32_bf16 v[12:15], v[244:247], v[88:91], v[12:15]\n\t"
    "v_mfma_f32_16x16x32_bf16 v[20:23], v[240:243], v[80:83], v[20:23]\n\t"
    "v_mfma_f32_16x16x32_bf16 v[20:23], v[244:247], v[92:95], v[20:23]\n\t"
    "s_nop 7\n\t"
    "v_cvt_pk_bf16_f32 v0, v0, v1\n\t"
    "v_cvt_pk_bf16_f32 v1, v2, v3\n\t"
    "v_cvt_pk_bf16_f32 v2, v4, v5\n\t"
    "v_cvt_pk_bf16_f32 v3, v6, v7\n\t"
    "s_lshl_b32 s30, s14, 10\n\t"
    "s_add_u32 s30, s30, 98304\n\t"
    "s_cmp_lg_u32 s15, 0\n\t"
    "s_cbranch_scc1 gqa_o101\n\t"
    "s_add_u32 s30, s14, 96\n\t"
    "s_lshr_b32 s30, s30, 4\n\t"
    "s_lshl_b32 s30, s30, 10\n\t"
    "s_add_u32 s30, s30, 156672\n\t"
    "v_add_u32 v150, s30, v252\n\t"
    "s_mov_b32 s30, 0x40000000\n\t"
    "ds_write_b128 v150, v[0:3]\n\t"
    "gqa_o101:\n\t"
    "buffer_store_dwordx4 v[0:3], v252, s[20:23], s30 offen sc1\n\t"
    "v_cvt_pk_bf16_f32 v8, v8, v9\n\t"
    "v_cvt_pk_bf16_f32 v9, v10, v11\n\t"
    "v_cvt_pk_bf16_f32 v10, v12, v13\n\t"
    "v_cvt_pk_bf16_f32 v11, v14, v15\n\t"
    "s_lshl_b32 s30, s14, 10\n\t"
    "s_add_u32 s30, s30, 99328\n\t"
    "s_cmp_lg_u32 s15, 15\n\t"
    "s_cbranch_scc1 gqa_o102\n\t"
    "s_add_u32 s30, s14, 97\n\t"
    "s_lshr_b32 s30, s30, 4\n\t"
    "s_lshl_b32 s30, s30, 10\n\t"
    "s_add_u32 s30, s30, 156672\n\t"
    "v_add_u32 v150, s30, v252\n\t"
    "s_mov_b32 s30, 0x40000000\n\t"
    "ds_write_b128 v150, v[8:11]\n\t"
    "gqa_o102:\n\t"
    "buffer_store_dwordx4 v[8:11], v252, s[20:23], s30 offen sc1\n\t"
    "v_cvt_pk_bf16_f32 v16, v16, v17\n\t"
    "v_cvt_pk_bf16_f32 v17, v18, v19\n\t"
    "v_cvt_pk_bf16_f32 v18, v20, v21\n\t"
    "v_cvt_pk_bf16_f32 v19, v22, v23\n\t"
    "s_lshl_b32 s30, s14, 10\n\t"
    "s_add_u32 s30, s30, 100352\n\t"
    "s_cmp_lg_u32 s15, 14\n\t"
    "s_cbranch_scc1 gqa_o103\n\t"
    "s_add_u32 s30, s14, 98\n\t"
    "s_lshr_b32 s30, s30, 4\n\t"
    "s_lshl_b32 s30, s30, 10\n\t"
    "s_add_u32 s30, s30, 156672\n\t"
    "v_add_u32 v150, s30, v252\n\t"
    "s_mov_b32 s30, 0x40000000\n\t"
    "ds_write_b128 v150, v[16:19]\n\t"
    "gqa_o103:\n\t"
    "buffer_store_dwordx4 v[16:19], v252, s[20:23], s30 offen sc1\n\t"
    "s_waitcnt lgkmcnt(0)\n\t"
    "v_mov_b32 v254, 1\n\t"
    "v_mov_b32 v255, 0\n\t"
    "s_waitcnt vmcnt(0)\n\t"
    "s_mov_b64 exec, 1\n\t"
    "buffer_atomic_add_x2 v[254:255], v147, s[24:27], 0 offen sc1\n\t"
    "s_mov_b64 exec, -1\n\t"
    "s_setprio 3\n\t"
    "s_add_u32 s14, s54, 4\n\t"
    "s_mov_b32 s55, -1\n\t"
    "s_mov_b32 s45, 0\n\t"
    "s_branch gqa_reduce\n\t"
    "gqa_cfb:\n\t"
    "s_nop 7\n\t"
    "s_nop 7\n\t"
    "s_waitcnt lgkmcnt(0)\n\t"
    "s_mov_b32 s62, 0\n\t"
    "s_mov_b32 s12, 0x10000\n\t"
    "s_lshl_b32 s30, s50, 4\n\t"
    "s_add_u32 s30, s30, s51\n\t"
    "s_lshl_b32 s30, s30, 17\n\t"
    "s_add_u32 s4, s42, s30\n\t"
    "s_addc_u32 s5, s43, 0\n\t"
    "s_and_b32 s5, s5, 0xffff\n\t"
    "s_mov_b32 s6, 131072\n\t"
    "s_mov_b32 s7, 0x20000\n\t"
    "v_mbcnt_lo_u32_b32 v3, -1, 0\n\t"
    "v_mbcnt_hi_u32_b32 v3, -1, v3\n\t"
    "v_lshrrev_b32 v0, 5, v3\n\t"
    "v_lshlrev_b32 v0, 10, v0\n\t"
    "v_bfe_u32 v1, v3, 2, 3\n\t"
    "v_lshl_add_u32 v0, v1, 7, v0\n\t"
    "v_and_b32 v1, 3, v3\n\t"
    "v_lshl_add_u32 v0, v1, 3, v0\n\t"
    "v_add_u32 v0, 0x10000, v0\n\t"
    "v_bfe_u32 v1, v3, 3, 2\n\t"
    "v_xor_b32 v2, 0, v1\n\t"
    "v_lshl_add_u32 v248, v2, 5, v0\n\t"
    "v_xor_b32 v2, 1, v1\n\t"
    "v_lshl_add_u32 v249, v2, 5, v0\n\t"
    "v_xor_b32 v2, 2, v1\n\t"
    "v_lshl_add_u32 v250, v2, 5, v0\n\t"
    "v_xor_b32 v2, 3, v1\n\t"
    "v_lshl_add_u32 v251, v2, 5, v0\n\t"
    "v_and_b32 v0, 15, v3\n\t"
    "v_lshrrev_b32 v1, 1, v0\n\t"
    "v_lshrrev_b32 v2, 4, v3\n\t"
    "v_xor_b32 v1, v1, v2\n\t"
    "v_lshlrev_b32 v0, 7, v0\n\t"
    "v_add_u32 v0, 0x10000, v0\n\t"
    "v_lshl_add_u32 v252, v1, 4, v0\n\t"
    "v_xor_b32 v1, 4, v1\n\t"
    "v_lshl_add_u32 v253, v1, 4, v0\n\t"
    "v_bfe_u32 v0, v3, 1, 1\n\t"
    "v_bfe_u32 v1, v3, 4, 1\n\t"
    "v_xor_b32 v0, v0, v1\n\t"
    "v_bfe_u32 v1, v3, 2, 1\n\t"
    "v_bfe_u32 v2, v3, 5, 1\n\t"
    "v_xor_b32 v1, v1, v2\n\t"
    "v_lshl_add_u32 v0, v0, 1, v1\n\t"
    "v_bfe_u32 v1, v3, 3, 1\n\t"
    "v_lshl_add_u32 v0, v1, 2, v0\n\t"
    "v_and_b32 v1, 15, v3\n\t"
    "v_lshlrev_b32 v1, 7, v1\n\t"
    "v_add_u32 v1, 0x10000, v1\n\t"
    "v_lshl_add_u32 v252, v0, 4, v1\n\t"
    "v_xor_b32 v253, 64, v252\n\t"
    "v_and_b32 v0, 15, v3\n\t"
    "v_lshlrev_b32 v0, 8, v0\n\t"
    "v_bfe_u32 v1, v3, 4, 1\n\t"
    "v_lshl_add_u32 v0, v1, 5, v0\n\t"
    "v_bfe_u32 v1, v3, 5, 1\n\t"
    "v_lshl_add_u32 v0, v1, 4, v0\n\t"
    "v_mov_b32 v132, 0xff800000\n\t"
    "v_mov_b32 v133, 0xff800000\n\t"
    "v_mov_b32 v134, 0xff800000\n\t"
    "v_mov_b32 v168, 0x3f803f80\n\t"
    "v_mov_b32 v169, 0x3f803f80\n\t"
    "v_mov_b32 v170, 0x3f803f80\n\t"
    "v_mov_b32 v171, 0x3f803f80\n\t"
    "s_mov_b32 s8, 122880\n\t"
    "12:\n\t"
    "s_add_u32 s9, s8, 4096\n\t"
    "buffer_load_dwordx4 v[4:7], v0, s[4:7], s8 offen offset:0\n\t"
    "buffer_load_dwordx4 v[8:11], v0, s[4:7], s8 offen offset:64\n\t"
    "buffer_load_dwordx4 v[12:15], v0, s[4:7], s8 offen offset:128\n\t"
    "buffer_load_dwordx4 v[16:19], v0, s[4:7], s8 offen offset:192\n\t"
    "buffer_load_dwordx4 v[20:23], v0, s[4:7], s9 offen offset:0\n\t"
    "buffer_load_dwordx4 v[24:27], v0, s[4:7], s9 offen offset:64\n\t"
    "buffer_load_dwordx4 v[28:31], v0, s[4:7], s9 offen offset:128\n\t"
    "buffer_load_dwordx4 v[32:35], v0, s[4:7], s9 offen offset:192\n\t"
    "s_waitcnt vmcnt(0)\n\t"
    "v_mfma_f32_16x16x32_bf16 v[96:99], v[4:7], v[184:187], 0\n\t"
    "v_mfma_f32_16x16x32_bf16 v[100:103], v[4:7], v[200:203], 0\n\t"
    "v_mfma_f32_16x16x32_bf16 v[104:107], v[4:7], v[216:219], 0\n\t"
    "v_mfma_f32_16x16x32_bf16 v[108:111], v[20:23], v[184:187], 0\n\t"
    "v_mfma_f32_16x16x32_bf16 v[112:115], v[20:23], v[200:203], 0\n\t"
    "v_mfma_f32_16x16x32_bf16 v[116:119], v[20:23], v[216:219], 0\n\t"
    "v_mfma_f32_16x16x32_bf16 v[96:99], v[8:11], v[188:191], v[96:99]\n\t"
    "v_mfma_f32_16x16x32_bf16 v[100:103], v[8:11], v[204:207], v[100:103]\n\t"
    "v_mfma_f32_16x16x32_bf16 v[104:107], v[8:11], v[220:223], v[104:107]\n\t"
    "v_mfma_f32_16x16x32_bf16 v[108:111], v[24:27], v[188:191], v[108:111]\n\t"
    "v_mfma_f32_16x16x32_bf16 v[112:115], v[24:27], v[204:207], v[112:115]\n\t"
    "v_mfma_f32_16x16x32_bf16 v[116:119], v[24:27], v[220:223], v[116:119]\n\t"
    "v_mfma_f32_16x16x32_bf16 v[96:99], v[12:15], v[192:195], v[96:99]\n\t"
    "v_mfma_f32_16x16x32_bf16 v[100:103], v[12:15], v[208:211], v[100:103]\n\t"
    "v_mfma_f32_16x16x32_bf16 v[104:107], v[12:15], v[224:227], v[104:107]\n\t"
    "v_mfma_f32_16x16x32_bf16 v[108:111], v[28:31], v[192:195], v[108:111]\n\t"
    "v_mfma_f32_16x16x32_bf16 v[112:115], v[28:31], v[208:211], v[112:115]\n\t"
    "v_mfma_f32_16x16x32_bf16 v[116:119], v[28:31], v[224:227], v[116:119]\n\t"
    "v_mfma_f32_16x16x32_bf16 v[96:99], v[16:19], v[196:199], v[96:99]\n\t"
    "v_mfma_f32_16x16x32_bf16 v[100:103], v[16:19], v[212:215], v[100:103]\n\t"
    "v_mfma_f32_16x16x32_bf16 v[104:107], v[16:19], v[228:231], v[104:107]\n\t"
    "v_mfma_f32_16x16x32_bf16 v[108:111], v[32:35], v[196:199], v[108:111]\n\t"
    "v_mfma_f32_16x16x32_bf16 v[112:115], v[32:35], v[212:215], v[112:115]\n\t"
    "v_mfma_f32_16x16x32_bf16 v[116:119], v[32:35], v[228:231], v[116:119]\n\t"
    "s_nop 7\n\t"
    "v_max3_f32 v132, v132, v96, v97\n\t"
    "v_max3_f32 v132, v132, v98, v99\n\t"
    "v_max3_f32 v132, v132, v108, v109\n\t"
    "v_max3_f32 v132, v132, v110, v111\n\t"
    "v_max3_f32 v133, v133, v100, v101\n\t"
    "v_max3_f32 v133, v133, v102, v103\n\t"
    "v_max3_f32 v133, v133, v112, v113\n\t"
    "v_max3_f32 v133, v133, v114, v115\n\t"
    "v_max3_f32 v134, v134, v104, v105\n\t"
    "v_max3_f32 v134, v134, v106, v107\n\t"
    "v_max3_f32 v134, v134, v116, v117\n\t"
    "v_max3_f32 v134, v134, v118, v119\n\t"
    "s_sub_u32 s8, s8, 8192\n\t"
    "s_cbranch_scc0 12b\n\t"
    "v_mov_b32 v135, v132\n\t"
    "v_mov_b32 v136, v133\n\t"
    "v_mov_b32 v137, v134\n\t"
    "v_permlane32_swap_b32 v132, v135\n\t"
    "v_permlane32_swap_b32 v133, v136\n\t"
    "v_permlane32_swap_b32 v134, v137\n\t"
    "v_max_f32 v132, v132, v135\n\t"
    "v_max_f32 v133, v133, v136\n\t"
    "v_max_f32 v134, v134, v137\n\t"
    "v_mov_b32 v135, v132\n\t"
    "v_mov_b32 v136, v133\n\t"
    "v_mov_b32 v137, v134\n\t"
    "v_permlane16_swap_b32 v132, v135\n\t"
    "v_permlane16_swap_b32 v133, v136\n\t"
    "v_permlane16_swap_b32 v134, v137\n\t"
    "v_max_f32 v132, v132, v135\n\t"
    "v_max_f32 v133, v133, v136\n\t"
    "v_max_f32 v134, v134, v137\n\t"
    "v_xor_b32 v144, 0x80000000, v132\n\t"
    "v_xor_b32 v145, 0x80000000, v132\n\t"
    "v_xor_b32 v146, 0x80000000, v132\n\t"
    "v_xor_b32 v147, 0x80000000, v132\n\t"
    "v_xor_b32 v148, 0x80000000, v133\n\t"
    "v_xor_b32 v149, 0x80000000, v133\n\t"
    "v_xor_b32 v150, 0x80000000, v133\n\t"
    "v_xor_b32 v151, 0x80000000, v133\n\t"
    "v_xor_b32 v152, 0x80000000, v134\n\t"
    "v_xor_b32 v153, 0x80000000, v134\n\t"
    "v_xor_b32 v154, 0x80000000, v134\n\t"
    "v_xor_b32 v155, 0x80000000, v134\n\t"
    "v_lshlrev_b32 v254, 4, v3\n\t"
    "v_add_u32 v255, s68, v254\n\t"
    "v_add_u32 v254, s67, v254\n\t"
    "s_barrier\n\t"
    "ds_read_b128 v[232:235], v252 offset:16384\n\t"
    "ds_read_b128 v[240:243], v253 offset:16384\n\t"
    "ds_read_b128 v[244:247], v252 offset:18432\n\t"
    "ds_read_b128 v[172:175], v253 offset:18432\n\t"
    "ds_read_b128 v[180:183], v252 offset:20480\n\t"
    "ds_read_b128 v[176:179], v253 offset:20480\n\t"
    "s_waitcnt lgkmcnt(0)\n\t"
    "s_branch gqa_att\n\t"
    "gqa_cend:\n\t"
    : : [ka]"s"(ka), [bid]"s"(bid), [wid]"s"(wid), [lds]"s"(ldsb) : "memory","vcc","scc","s4","s5","s6","s7","s8","s9","s10","s11","s12","s13","s14","s15","s16","s17","s18","s19","s20","s21","s22","s23","s24","s25","s26","s27","s28","s29","s30","s31","s32","s33","s34","s35","s36","s37","s38","s39","s40","s41","s42","s43","s44","s45","s46","s47","s48","s49","s50","s51","s52","s53","s54","s55","s56","s57","s58","s59","s60","s61","s62","s63","s64","s65","s66","s67","s68","s69","v0","v1","v2","v3","v4","v5","v6","v7","v8","v9","v10","v11","v12","v13","v14","v15","v16","v17","v18","v19","v20","v21","v22","v23","v24","v25","v26","v27","v28","v29","v30","v31","v32","v33","v34","v35","v36","v37","v38","v39","v40","v41","v42","v43","v44","v45","v46","v47","v48","v49","v50","v51","v52","v53","v54","v55","v56","v57","v58","v59","v60","v61","v62","v63","v64","v65","v66","v67","v68","v69","v70","v71","v72","v73","v74","v75","v76","v77","v78","v79","v80","v81","v82","v83","v84","v85","v86","v87","v88","v89","v90","v91","v92","v93","v94","v95","v96","v97","v98","v99","v100","v101","v102","v103","v104","v105","v106","v107","v108","v109","v110","v111","v112","v113","v114","v115","v116","v117","v118","v119","v120","v121","v122","v123","v124","v125","v126","v127","v128","v129","v130","v131","v132","v133","v134","v135","v136","v137","v138","v139","v140","v141","v142","v143","v144","v145","v146","v147","v148","v149","v150","v151","v152","v153","v154","v155","v156","v157","v158","v159","v160","v161","v162","v163","v164","v165","v166","v167","v168","v169","v170","v171","v172","v173","v174","v175","v176","v177","v178","v179","v180","v181","v182","v183","v184","v185","v186","v187","v188","v189","v190","v191","v192","v193","v194","v195","v196","v197","v198","v199","v200","v201","v202","v203","v204","v205","v206","v207","v208","v209","v210","v211","v212","v213","v214","v215","v216","v217","v218","v219","v220","v221","v222","v223","v224","v225","v226","v227","v228","v229","v230","v231","v232","v233","v234","v235","v236","v237","v238","v239","v240","v241","v242","v243","v244","v245","v246","v247","v248","v249","v250","v251","v252","v253","v254","v255");
  } else {
    asm volatile(
    "gqa_lstart:\n\t"
    "s_load_dwordx8 s[40:47], %[ka], 0x0\n\t"
    "s_load_dwordx2 s[48:49], %[ka], 0x20\n\t"
    "s_load_dwordx2 s[58:59], %[ka], 0x28\n\t"
    "s_load_dwordx2 s[56:57], %[ka], 0x30\n\t"
    "s_mov_b32 s54, %[wid]\n\t"
    "s_mov_b32 s30, %[bid]\n\t"
    "s_lshr_b32 s53, s30, 5\n\t"
    "s_bfe_u32 s31, s30, 0x20003\n\t"
    "s_bfe_u32 s51, s30, 0x20001\n\t"
    "s_and_b32 s50, s30, 1\n\t"
    "s_lshl_b32 s50, s50, 1\n\t"
    "s_xor_b32 s51, s51, s50\n\t"
    "s_lshl_b32 s51, s51, 2\n\t"
    "s_add_u32 s51, s51, s31\n\t"
    "s_and_b32 s50, s30, 1\n\t"
    "s_lshl_b32 s50, s50, 3\n\t"
    "s_add_u32 s50, s50, s53\n\t"
    "s_lshl_b32 s31, s31, 2\n\t"
    "s_lshr_b32 s52, 0x7420, s31\n\t"
    "s_and_b32 s52, s52, 15\n\t"
    "s_add_u32 s52, s52, s53\n\t"
    "s_cmp_ge_u32 s52, 9\n\t"
    "s_cselect_b32 s31, 9, 0\n\t"
    "s_sub_u32 s52, s52, s31\n\t"
    "s_sub_u32 s55, s54, 4\n\t"
    "v_mbcnt_lo_u32_b32 v1, -1, 0\n\t"
    "v_mbcnt_hi_u32_b32 v1, -1, v1\n\t"
    "v_lshrrev_b32 v20, 3, v1\n\t"
    "v_and_b32 v21, 7, v1\n\t"
    "v_bfe_u32 v22, v1, 4, 2\n\t"
    "v_xor_b32 v24, v21, v22\n\t"
    "v_lshlrev_b32 v24, 4, v24\n\t"
    "v_mul_u32_u24 v25, 1152, v20\n\t"
    "s_mul_i32 s30, s55, 27648\n\t"
    "s_and_b32 s31, s55, 1\n\t"
    "s_lshl_b32 s31, s31, 6\n\t"
    "v_xor_b32 v26, s31, v24\n\t"
    "v_add3_u32 v27, v25, v26, s30\n\t"
    "v_xor_b32 v26, 64, v26\n\t"
    "v_add3_u32 v28, v25, v26, s30\n\t"
    "v_add_u32 v6, 4096, v27\n\t"
    "v_add_u32 v7, 12288, v28\n\t"
    "v_add_u32 v8, 20480, v27\n\t"
    "v_lshlrev_b32 v23, 1, v22\n\t"
    "v_xor_b32 v23, v21, v23\n\t"
    "v_lshlrev_b32 v23, 4, v23\n\t"
    "v_lshrrev_b32 v26, 2, v20\n\t"
    "v_and_b32 v27, 3, v20\n\t"
    "v_lshl_add_u32 v26, v26, 3, v27\n\t"
    "v_lshlrev_b32 v26, 13, v26\n\t"
    "s_lshl_b32 s30, s51, 9\n\t"
    "s_lshr_b32 s31, s55, 1\n\t"
    "s_lshl_b32 s31, s31, 8\n\t"
    "s_add_u32 s30, s30, s31\n\t"
    "s_and_b32 s31, s55, 1\n\t"
    "s_lshl_b32 s31, s31, 18\n\t"
    "s_add_u32 s30, s30, s31\n\t"
    "v_add3_u32 v26, v26, v23, s30\n\t"
    "v_add_u32 v2, 4096, v26\n\t"
    "v_add_u32 v3, 134144, v26\n\t"
    "v_add_u32 v4, 34816, v26\n\t"
    "v_add_u32 v5, 164864, v26\n\t"
    "s_mul_i32 s37, s55, 3072\n\t"
    "s_add_u32 s37, s37, 126976\n\t"
    "s_lshl_b32 s38, s52, 7\n\t"
    "s_lshl_b32 s39, s52, 7\n\t"
    "s_lshl_b32 s36, s55, 12\n\t"
    "s_mov_b32 s28, 0\n\t"
    "s_mov_b32 s29, 0x8000\n\t"
    "s_mov_b32 s68, 0\n\t"
    "s_mov_b32 s65, 0x10000\n\t"
    "s_mul_i32 s30, s50, 110592\n\t"
    "s_waitcnt lgkmcnt(0)\n\t"
    "s_add_u32 s4, s40, s30\n\t"
    "s_addc_u32 s5, s41, 0\n\t"
    "s_sub_u32 s4, s4, 4096\n\t"
    "s_subb_u32 s5, s5, 0\n\t"
    "s_and_b32 s5, s5, 0xffff\n\t"
    "s_mov_b32 s6, 114688\n\t"
    "s_mov_b32 s7, 0x20000\n\t"
    "s_sub_u32 s8, s46, 4096\n\t"
    "s_subb_u32 s9, s47, 0\n\t"
    "s_and_b32 s9, s9, 0xffff\n\t"
    "s_mov_b32 s10, 4722688\n\t"
    "s_mov_b32 s11, 0x20000\n\t"
    "s_add_u32 m0, s37, s28\n\t"
    "s_nop 0\n\t"
    "buffer_load_dwordx4 v6, s[4:7], s38 offen lds\n\t"
    "buffer_load_dwordx4 v7, s[4:7], s38 offen offset:1024 lds\n\t"
    "buffer_load_dwordx4 v8, s[4:7], s38 offen offset:2048 lds\n\t"
    "s_add_u32 s38, s38, 128\n\t"
    "s_cmp_eq_u32 s38, 1152\n\t"
    "s_cselect_b32 s38, 0, s38\n\t"
    "s_add_u32 s28, s28, 12288\n\t"
    "s_cmp_eq_u32 s28, 36864\n\t"
    "s_cselect_b32 s28, 0, s28\n\t"
    "s_lshl_b32 s30, s39, 12\n\t"
    "s_add_u32 m0, s36, s29\n\t"
    "s_nop 0\n\t"
    "buffer_load_dwordx4 v2, s[8:11], s30 offen lds\n\t"
    "buffer_load_dwordx4 v3, s[8:11], s30 offen offset:1024 lds\n\t"
    "buffer_load_dwordx4 v4, s[8:11], s30 offen offset:2048 lds\n\t"
    "buffer_load_dwordx4 v5, s[8:11], s30 offen offset:3072 lds\n\t"
    "s_add_u32 s29, s29, 16384\n\t"
    "s_and_b32 s29, s29, 0xffff\n\t"
    "s_lshl_b32 s31, s50, 7\n\t"
    "s_load_dwordx2 s[32:33], s[56:57], s31\n\t"
    "s_mov_b32 s24, s56\n\t"
    "s_mov_b32 s25, s57\n\t"
    "s_and_b32 s25, s25, 0xffff\n\t"
    "s_mov_b32 s26, 1048576\n\t"
    "s_mov_b32 s27, 0x20000\n\t"
    "s_getpc_b64 s[60:61]\n\t"
    "gqa_pc2:\n\t"
    "s_sub_u32 s60, s60, gqa_pc2-gqa_lstart\n\t"
    "s_subb_u32 s61, s61, 0\n\t"
    "s_and_b32 s61, s61, 0xffff\n\t"
    "s_mov_b32 s62, gqa_cend-gqa_lstart\n\t"
    "s_mov_b32 s63, 0x20000\n\t"
    "s_lshr_b32 s64, %[bid], 3\n\t"
    "s_lshl_b32 s64, s64, 2\n\t"
    "s_add_u32 s64, s64, s55\n\t"
    "s_lshl_b32 s64, s64, 9\n\t"
    "v_lshlrev_b32 v60, 3, v1\n\t"
    "buffer_load_dwordx2 v[62:63], v60, s[60:63], s64 offen\n\t"
    "s_lshl_b32 s30, s50, 4\n\t"
    "s_add_u32 s30, s30, s51\n\t"
    "s_lshl_b32 s30, s30, 17\n\t"
    "s_cmp_lt_u32 s55, 2\n\t"
    "s_cselect_b32 s42, s42, s44\n\t"
    "s_cselect_b32 s43, s43, s45\n\t"
    "s_add_u32 s12, s42, s30\n\t"
    "s_addc_u32 s13, s43, 0\n\t"
    "s_sub_u32 s12, s12, 4096\n\t"
    "s_subb_u32 s13, s13, 0\n\t"
    "s_and_b32 s13, s13, 0xffff\n\t"
    "s_mov_b32 s14, 135168\n\t"
    "s_mov_b32 s15, 0x20000\n\t"
    "s_sub_u32 s16, s48, 4096\n\t"
    "s_subb_u32 s17, s49, 0\n\t"
    "s_and_b32 s17, s17, 0xffff\n\t"
    "s_mov_b32 s18, 4722688\n\t"
    "s_mov_b32 s19, 0x20000\n\t"
    "v_lshlrev_b32 v26, 8, v20\n\t"
    "s_cmp_lt_u32 s55, 2\n\t"
    "s_cbranch_scc0 1f\n\t"
    "s_lshl_b32 s30, s55, 12\n\t"
    "v_bfe_u32 v27, v1, 4, 1\n\t"
    "v_bfe_u32 v28, v1, 5, 1\n\t"
    "v_lshl_add_u32 v27, v27, 1, v28\n\t"
    "v_xor_b32 v29, v21, v27\n\t"
    "v_lshlrev_b32 v29, 4, v29\n\t"
    "v_xor_b32 v27, 0, v29\n\t"
    "v_add3_u32 v27, v26, v27, s30\n\t"
    "v_add_u32 v9, 4096, v27\n\t"
    "v_xor_b32 v27, 64, v29\n\t"
    "v_add3_u32 v27, v26, v27, s30\n\t"
    "v_add_u32 v10, 5120, v27\n\t"
    "v_xor_b32 v27, 0, v29\n\t"
    "v_add3_u32 v27, v26, v27, s30\n\t"
    "v_add_u32 v11, 2176, v27\n\t"
    "v_xor_b32 v27, 64, v29\n\t"
    "v_add3_u32 v27, v26, v27, s30\n\t"
    "v_add_u32 v12, 3200, v27\n\t"
    "s_branch 2f\n\t"
    "1:\n\t"
    "s_sub_u32 s30, s55, 2\n\t"
    "s_lshl_b32 s30, s30, 7\n\t"
    "v_add3_u32 v27, v26, v23, s30\n\t"
    "v_add_u32 v9, 4096, v27\n\t"
    "v_add_u32 v10, 5120, v27\n\t"
    "v_add_u32 v11, 6144, v27\n\t"
    "v_add_u32 v12, 7168, v27\n\t"
    "2:\n\t"
    "s_lshl_b32 s30, s51, 8\n\t"
    "s_lshl_b32 s31, s55, 5\n\t"
    "s_add_u32 s30, s30, s31\n\t"
    "s_mul_i32 s30, s30, 1152\n\t"
    "v_add3_u32 v27, v25, v23, s30\n\t"
    "v_add_u32 v13, 4096, v27\n\t"
    "v_add_u32 v14, 12288, v27\n\t"
    "v_add_u32 v15, 20480, v27\n\t"
    "v_add_u32 v16, 28672, v27\n\t"
    "s_lshl_b32 s35, s53, 7\n\t"
    "s_lshl_b32 s30, s50, 4\n\t"
    "s_add_u32 s30, s30, s51\n\t"
    "s_mul_i32 s30, s30, 110592\n\t"
    "s_add_u32 s20, s56, s30\n\t"
    "s_addc_u32 s21, s57, 0\n\t"
    "s_add_u32 s20, s20, 1048576\n\t"
    "s_addc_u32 s21, s21, 0\n\t"
    "s_and_b32 s21, s21, 0xffff\n\t"
    "s_mov_b32 s22, 110592\n\t"
    "s_mov_b32 s23, 0x20000\n\t"
    "s_mul_i32 s69, s55, 3072\n\t"
    "s_mul_i32 s46, s55, 3\n\t"
    "s_sub_u32 s47, s46, s51\n\t"
    "s_and_b32 s47, s47, 15\n\t"
    "v_lshlrev_b32 v17, 4, v1\n\t"
    "s_add_u32 s30, s69, 131072\n\t"
    "v_add_u32 v18, s30, v17\n\t"
    "s_lshl_b32 s66, s50, 2\n\t"
    "s_add_u32 s66, s66, s55\n\t"
    "s_lshl_b32 s66, s66, 4\n\t"
    "s_add_u32 s66, s66, s51\n\t"
    "s_lshl_b32 s66, s66, 7\n\t"
    "s_add_u32 s66, s66, 4096\n\t"
    "v_cmp_eq_u32 vcc, 0, v1\n\t"
    "v_mov_b32 v28, s66\n\t"
    "v_bfrev_b32 v19, -2\n\t"
    "v_cndmask_b32 v19, v19, v28, vcc\n\t"
    "s_lshl_b32 s30, s39, 12\n\t"
    "s_add_u32 s30, s30, 128\n\t"
    "s_add_u32 m0, s36, s29\n\t"
    "s_nop 0\n\t"
    "buffer_load_dwordx4 v2, s[8:11], s30 offen lds\n\t"
    "buffer_load_dwordx4 v3, s[8:11], s30 offen offset:1024 lds\n\t"
    "buffer_load_dwordx4 v4, s[8:11], s30 offen offset:2048 lds\n\t"
    "buffer_load_dwordx4 v5, s[8:11], s30 offen offset:3072 lds\n\t"
    "s_add_u32 s29, s29, 16384\n\t"
    "s_and_b32 s29, s29, 0xffff\n\t"
    "s_add_u32 s39, s39, 128\n\t"
    "s_cmp_eq_u32 s39, 1152\n\t"
    "s_cselect_b32 s39, 0, s39\n\t"
    "s_add_u32 s67, s53, 3\n\t"
    "s_cmp_ge_u32 s67, 9\n\t"
    "s_cselect_b32 s30, 9, 0\n\t"
    "s_sub_u32 s67, s67, s30\n\t"
    "s_lshl_b32 s67, s67, 7\n\t"
    "s_waitcnt vmcnt(5)\n\t"
    "s_barrier\n\t"
    "s_add_u32 m0, s37, s28\n\t"
    "s_nop 0\n\t"
    "buffer_load_dwordx4 v6, s[4:7], s38 offen lds\n\t"
    "buffer_load_dwordx4 v7, s[4:7], s38 offen offset:1024 lds\n\t"
    "buffer_load_dwordx4 v8, s[4:7], s38 offen offset:2048 lds\n\t"
    "s_add_u32 s38, s38, 128\n\t"
    "s_cmp_eq_u32 s38, 1152\n\t"
    "s_cselect_b32 s38, 0, s38\n\t"
    "s_add_u32 s28, s28, 12288\n\t"
    "s_cmp_eq_u32 s28, 36864\n\t"
    "s_cselect_b32 s28, 0, s28\n\t"
    "s_lshl_b32 s30, s39, 12\n\t"
    "s_add_u32 m0, s36, s29\n\t"
    "s_nop 0\n\t"
    "buffer_load_dwordx4 v2, s[8:11], s30 offen lds\n\t"
    "buffer_load_dwordx4 v3, s[8:11], s30 offen offset:1024 lds\n\t"
    "buffer_load_dwordx4 v4, s[8:11], s30 offen offset:2048 lds\n\t"
    "buffer_load_dwordx4 v5, s[8:11], s30 offen offset:3072 lds\n\t"
    "s_add_u32 s29, s29, 16384\n\t"
    "s_and_b32 s29, s29, 0xffff\n\t"
    "s_add_u32 m0, s37, s28\n\t"
    "s_nop 0\n\t"
    "buffer_load_dwordx4 v6, s[4:7], s38 offen lds\n\t"
    "buffer_load_dwordx4 v7, s[4:7], s38 offen offset:1024 lds\n\t"
    "buffer_load_dwordx4 v8, s[4:7], s38 offen offset:2048 lds\n\t"
    "s_add_u32 s38, s38, 128\n\t"
    "s_cmp_eq_u32 s38, 1152\n\t"
    "s_cselect_b32 s38, 0, s38\n\t"
    "s_add_u32 s28, s28, 12288\n\t"
    "s_cmp_eq_u32 s28, 36864\n\t"
    "s_cselect_b32 s28, 0, s28\n\t"
    "s_waitcnt vmcnt(10)\n\t"
    "s_barrier\n\t"
    "s_lshl_b32 s30, s39, 12\n\t"
    "s_add_u32 s30, s30, 128\n\t"
    "s_add_u32 m0, s36, s29\n\t"
    "s_nop 0\n\t"
    "buffer_load_dwordx4 v2, s[8:11], s30 offen lds\n\t"
    "buffer_load_dwordx4 v3, s[8:11], s30 offen offset:1024 lds\n\t"
    "buffer_load_dwordx4 v4, s[8:11], s30 offen offset:2048 lds\n\t"
    "buffer_load_dwordx4 v5, s[8:11], s30 offen offset:3072 lds\n\t"
    "s_add_u32 s29, s29, 16384\n\t"
    "s_and_b32 s29, s29, 0xffff\n\t"
    "s_add_u32 s39, s39, 128\n\t"
    "s_cmp_eq_u32 s39, 1152\n\t"
    "s_cselect_b32 s39, 0, s39\n\t"
    "s_lshl_b32 s30, s39, 12\n\t"
    "s_add_u32 m0, s36, s29\n\t"
    "s_nop 0\n\t"
    "buffer_load_dwordx4 v2, s[8:11], s30 offen lds\n\t"
    "buffer_load_dwordx4 v3, s[8:11], s30 offen offset:1024 lds\n\t"
    "buffer_load_dwordx4 v4, s[8:11], s30 offen offset:2048 lds\n\t"
    "buffer_load_dwordx4 v5, s[8:11], s30 offen offset:3072 lds\n\t"
    "s_add_u32 s29, s29, 16384\n\t"
    "s_and_b32 s29, s29, 0xffff\n\t"
    "s_waitcnt vmcnt(11)\n\t"
    "s_barrier\n\t"
    "s_lshl_b32 s30, s39, 12\n\t"
    "s_add_u32 s30, s30, 128\n\t"
    "s_add_u32 m0, s36, s29\n\t"
    "s_nop 0\n\t"
    "buffer_load_dwordx4 v2, s[8:11], s30 offen lds\n\t"
    "buffer_load_dwordx4 v3, s[8:11], s30 offen offset:1024 lds\n\t"
    "buffer_load_dwordx4 v4, s[8:11], s30 offen offset:2048 lds\n\t"
    "buffer_load_dwordx4 v5, s[8:11], s30 offen offset:3072 lds\n\t"
    "s_add_u32 s29, s29, 16384\n\t"
    "s_and_b32 s29, s29, 0xffff\n\t"
    "s_add_u32 s39, s39, 128\n\t"
    "s_cmp_eq_u32 s39, 1152\n\t"
    "s_cselect_b32 s39, 0, s39\n\t"
    "s_add_u32 m0, s37, s28\n\t"
    "s_nop 0\n\t"
    "buffer_load_dwordx4 v6, s[4:7], s38 offen lds\n\t"
    "buffer_load_dwordx4 v7, s[4:7], s38 offen offset:1024 lds\n\t"
    "buffer_load_dwordx4 v8, s[4:7], s38 offen offset:2048 lds\n\t"
    "s_add_u32 s38, s38, 128\n\t"
    "s_cmp_eq_u32 s38, 1152\n\t"
    "s_cselect_b32 s38, 0, s38\n\t"
    "s_add_u32 s28, s28, 12288\n\t"
    "s_cmp_eq_u32 s28, 36864\n\t"
    "s_cselect_b32 s28, 0, s28\n\t"
    "s_waitcnt vmcnt(11)\n\t"
    "s_barrier\n\t"
    "s_lshl_b32 s30, s39, 12\n\t"
    "s_add_u32 m0, s36, s29\n\t"
    "s_nop 0\n\t"
    "buffer_load_dwordx4 v2, s[8:11], s30 offen lds\n\t"
    "buffer_load_dwordx4 v3, s[8:11], s30 offen offset:1024 lds\n\t"
    "buffer_load_dwordx4 v4, s[8:11], s30 offen offset:2048 lds\n\t"
    "buffer_load_dwordx4 v5, s[8:11], s30 offen offset:3072 lds\n\t"
    "s_add_u32 s29, s29, 16384\n\t"
    "s_and_b32 s29, s29, 0xffff\n\t"
    "s_add_u32 m0, s36, s65\n\t"
    "s_nop 0\n\t"
    "buffer_load_dwordx4 v9, s[12:15], s68 offen nt lds\n\t"
    "buffer_load_dwordx4 v10, s[12:15], s68 offen offset:1024 nt lds\n\t"
    "buffer_load_dwordx4 v11, s[12:15], s68 offen offset:2048 nt lds\n\t"
    "buffer_load_dwordx4 v12, s[12:15], s68 offen offset:3072 nt lds\n\t"
    "s_add_u32 s68, s68, 8192\n\t"
    "s_add_u32 s65, s65, 16384\n\t"
    "s_and_b32 s65, s65, 0x1ffff\n\t"
    "s_waitcnt vmcnt(15)\n\t"
    "s_barrier\n\t"
    "s_lshl_b32 s30, s39, 12\n\t"
    "s_add_u32 s30, s30, 128\n\t"
    "s_add_u32 m0, s36, s29\n\t"
    "s_nop 0\n\t"
    "buffer_load_dwordx4 v2, s[8:11], s30 offen lds\n\t"
    "buffer_load_dwordx4 v3, s[8:11], s30 offen offset:1024 lds\n\t"
    "buffer_load_dwordx4 v4, s[8:11], s30 offen offset:2048 lds\n\t"
    "buffer_load_dwordx4 v5, s[8:11], s30 offen offset:3072 lds\n\t"
    "s_add_u32 s29, s29, 16384\n\t"
    "s_and_b32 s29, s29, 0xffff\n\t"
    "s_add_u32 s39, s39, 128\n\t"
    "s_cmp_eq_u32 s39, 1152\n\t"
    "s_cselect_b32 s39, 0, s39\n\t"
    "s_add_u32 m0, s37, s28\n\t"
    "s_nop 0\n\t"
    "buffer_load_dwordx4 v6, s[4:7], s38 offen lds\n\t"
    "buffer_load_dwordx4 v7, s[4:7], s38 offen offset:1024 lds\n\t"
    "buffer_load_dwordx4 v8, s[4:7], s38 offen offset:2048 lds\n\t"
    "s_add_u32 s38, s38, 128\n\t"
    "s_cmp_eq_u32 s38, 1152\n\t"
    "s_cselect_b32 s38, 0, s38\n\t"
    "s_add_u32 s28, s28, 12288\n\t"
    "s_cmp_eq_u32 s28, 36864\n\t"
    "s_cselect_b32 s28, 0, s28\n\t"
    "s_waitcnt vmcnt(18)\n\t"
    "s_barrier\n\t"
    "s_lshl_b32 s30, s39, 12\n\t"
    "s_add_u32 m0, s36, s29\n\t"
    "s_nop 0\n\t"
    "buffer_load_dwordx4 v2, s[8:11], s30 offen lds\n\t"
    "buffer_load_dwordx4 v3, s[8:11], s30 offen offset:1024 lds\n\t"
    "buffer_load_dwordx4 v4, s[8:11], s30 offen offset:2048 lds\n\t"
    "buffer_load_dwordx4 v5, s[8:11], s30 offen offset:3072 lds\n\t"
    "s_add_u32 s29, s29, 16384\n\t"
    "s_and_b32 s29, s29, 0xffff\n\t"
    "s_waitcnt vmcnt(15)\n\t"
    "s_barrier\n\t"
    "s_lshl_b32 s30, s39, 12\n\t"
    "s_add_u32 s30, s30, 128\n\t"
    "s_add_u32 m0, s36, s29\n\t"
    "s_nop 0\n\t"
    "buffer_load_dwordx4 v2, s[8:11], s30 offen lds\n\t"
    "buffer_load_dwordx4 v3, s[8:11], s30 offen offset:1024 lds\n\t"
    "buffer_load_dwordx4 v4, s[8:11], s30 offen offset:2048 lds\n\t"
    "buffer_load_dwordx4 v5, s[8:11], s30 offen offset:3072 lds\n\t"
    "s_add_u32 s29, s29, 16384\n\t"
    "s_and_b32 s29, s29, 0xffff\n\t"
    "s_add_u32 s39, s39, 128\n\t"
    "s_cmp_eq_u32 s39, 1152\n\t"
    "s_cselect_b32 s39, 0, s39\n\t"
    "s_add_u32 m0, s37, s28\n\t"
    "s_nop 0\n\t"
    "buffer_load_dwordx4 v6, s[4:7], s38 offen lds\n\t"
    "buffer_load_dwordx4 v7, s[4:7], s38 offen offset:1024 lds\n\t"
    "buffer_load_dwordx4 v8, s[4:7], s38 offen offset:2048 lds\n\t"
    "s_add_u32 s38, s38, 128\n\t"
    "s_cmp_eq_u32 s38, 1152\n\t"
    "s_cselect_b32 s38, 0, s38\n\t"
    "s_add_u32 s28, s28, 12288\n\t"
    "s_cmp_eq_u32 s28, 36864\n\t"
    "s_cselect_b32 s28, 0, s28\n\t"
    "s_add_u32 m0, s36, s65\n\t"
    "s_nop 0\n\t"
    "buffer_load_dwordx4 v9, s[12:15], s68 offen nt lds\n\t"
    "buffer_load_dwordx4 v10, s[12:15], s68 offen offset:1024 nt lds\n\t"
    "buffer_load_dwordx4 v11, s[12:15], s68 offen offset:2048 nt lds\n\t"
    "buffer_load_dwordx4 v12, s[12:15], s68 offen offset:3072 nt lds\n\t"
    "s_add_u32 s68, s68, 8192\n\t"
    "s_add_u32 s65, s65, 16384\n\t"
    "s_and_b32 s65, s65, 0x1ffff\n\t"
    "s_waitcnt vmcnt(18)\n\t"
    "s_barrier\n\t"
    "s_lshl_b32 s30, s39, 12\n\t"
    "s_add_u32 m0, s36, s29\n\t"
    "s_nop 0\n\t"
    "buffer_load_dwordx4 v2, s[8:11], s30 offen lds\n\t"
    "buffer_load_dwordx4 v3, s[8:11], s30 offen offset:1024 lds\n\t"
    "buffer_load_dwordx4 v4, s[8:11], s30 offen offset:2048 lds\n\t"
    "buffer_load_dwordx4 v5, s[8:11], s30 offen offset:3072 lds\n\t"
    "s_add_u32 s29, s29, 16384\n\t"
    "s_and_b32 s29, s29, 0xffff\n\t"
    "s_waitcnt vmcnt(15)\n\t"
    "s_barrier\n\t"
    "s_lshl_b32 s30, s39, 12\n\t"
    "s_add_u32 s30, s30, 128\n\t"
    "s_add_u32 m0, s36, s29\n\t"
    "s_nop 0\n\t"
    "buffer_load_dwordx4 v2, s[8:11], s30 offen lds\n\t"
    "buffer_load_dwordx4 v3, s[8:11], s30 offen offset:1024 lds\n\t"
    "buffer_load_dwordx4 v4, s[8:11], s30 offen offset:2048 lds\n\t"
    "buffer_load_dwordx4 v5, s[8:11], s30 offen offset:3072 lds\n\t"
    "s_add_u32 s29, s29, 16384\n\t"
    "s_and_b32 s29, s29, 0xffff\n\t"
    "s_add_u32 s39, s39, 128\n\t"
    "s_cmp_eq_u32 s39, 1152\n\t"
    "s_cselect_b32 s39, 0, s39\n\t"
    "s_add_u32 m0, s37, s28\n\t"
    "s_nop 0\n\t"
    "buffer_load_dwordx4 v6, s[4:7], s38 offen lds\n\t"
    "buffer_load_dwordx4 v7, s[4:7], s38 offen offset:1024 lds\n\t"
    "buffer_load_dwordx4 v8, s[4:7], s38 offen offset:2048 lds\n\t"
    "s_add_u32 s38, s38, 128\n\t"
    "s_cmp_eq_u32 s38, 1152\n\t"
    "s_cselect_b32 s38, 0, s38\n\t"
    "s_add_u32 s28, s28, 12288\n\t"
    "s_cmp_eq_u32 s28, 36864\n\t"
    "s_cselect_b32 s28, 0, s28\n\t"
    "s_waitcnt vmcnt(18)\n\t"
    "s_barrier\n\t"
    "s_lshl_b32 s30, s39, 12\n\t"
    "s_add_u32 m0, s36, s29\n\t"
    "s_nop 0\n\t"
    "buffer_load_dwordx4 v2, s[8:11], s30 offen lds\n\t"
    "buffer_load_dwordx4 v3, s[8:11], s30 offen offset:1024 lds\n\t"
    "buffer_load_dwordx4 v4, s[8:11], s30 offen offset:2048 lds\n\t"
    "buffer_load_dwordx4 v5, s[8:11], s30 offen offset:3072 lds\n\t"
    "s_add_u32 s29, s29, 16384\n\t"
    "s_and_b32 s29, s29, 0xffff\n\t"
    "s_add_u32 m0, s36, s65\n\t"
    "s_nop 0\n\t"
    "buffer_load_dwordx4 v9, s[12:15], s68 offen nt lds\n\t"
    "buffer_load_dwordx4 v10, s[12:15], s68 offen offset:1024 nt lds\n\t"
    "buffer_load_dwordx4 v11, s[12:15], s68 offen offset:2048 nt lds\n\t"
    "buffer_load_dwordx4 v12, s[12:15], s68 offen offset:3072 nt lds\n\t"
    "s_add_u32 s68, s68, 8192\n\t"
    "s_add_u32 s65, s65, 16384\n\t"
    "s_and_b32 s65, s65, 0x1ffff\n\t"
    "s_waitcnt vmcnt(15)\n\t"
    "s_barrier\n\t"
    "s_lshl_b32 s30, s39, 12\n\t"
    "s_add_u32 s30, s30, 128\n\t"
    "s_add_u32 m0, s36, s29\n\t"
    "s_nop 0\n\t"
    "buffer_load_dwordx4 v2, s[8:11], s30 offen lds\n\t"
    "buffer_load_dwordx4 v3, s[8:11], s30 offen offset:1024 lds\n\t"
    "buffer_load_dwordx4 v4, s[8:11], s30 offen offset:2048 lds\n\t"
    "buffer_load_dwordx4 v5, s[8:11], s30 offen offset:3072 lds\n\t"
    "s_add_u32 s29, s29, 16384\n\t"
    "s_and_b32 s29, s29, 0xffff\n\t"
    "s_add_u32 s39, s39, 128\n\t"
    "s_cmp_eq_u32 s39, 1152\n\t"
    "s_cselect_b32 s39, 0, s39\n\t"
    "s_add_u32 m0, s37, s28\n\t"
    "s_nop 0\n\t"
    "buffer_load_dwordx4 v6, s[4:7], s38 offen lds\n\t"
    "buffer_load_dwordx4 v7, s[4:7], s38 offen offset:1024 lds\n\t"
    "buffer_load_dwordx4 v8, s[4:7], s38 offen offset:2048 lds\n\t"
    "s_add_u32 s38, s38, 128\n\t"
    "s_cmp_eq_u32 s38, 1152\n\t"
    "s_cselect_b32 s38, 0, s38\n\t"
    "s_add_u32 s28, s28, 12288\n\t"
    "s_cmp_eq_u32 s28, 36864\n\t"
    "s_cselect_b32 s28, 0, s28\n\t"
    "s_waitcnt vmcnt(18)\n\t"
    "s_barrier\n\t"
    "s_lshl_b32 s30, s39, 12\n\t"
    "s_add_u32 m0, s36, s29\n\t"
    "s_nop 0\n\t"
    "buffer_load_dwordx4 v2, s[8:11], s30 offen lds\n\t"
    "buffer_load_dwordx4 v3, s[8:11], s30 offen offset:1024 lds\n\t"
    "buffer_load_dwordx4 v4, s[8:11], s30 offen offset:2048 lds\n\t"
    "buffer_load_dwordx4 v5, s[8:11], s30 offen offset:3072 lds\n\t"
    "s_add_u32 s29, s29, 16384\n\t"
    "s_and_b32 s29, s29, 0xffff\n\t"
    "s_waitcnt vmcnt(15)\n\t"
    "s_barrier\n\t"
    "s_lshl_b32 s30, s39, 12\n\t"
    "s_add_u32 s30, s30, 128\n\t"
    "s_add_u32 m0, s36, s29\n\t"
    "s_nop 0\n\t"
    "buffer_load_dwordx4 v2, s[8:11], s30 offen lds\n\t"
    "buffer_load_dwordx4 v3, s[8:11], s30 offen offset:1024 lds\n\t"
    "buffer_load_dwordx4 v4, s[8:11], s30 offen offset:2048 lds\n\t"
    "buffer_load_dwordx4 v5, s[8:11], s30 offen offset:3072 lds\n\t"
    "s_add_u32 s29, s29, 16384\n\t"
    "s_and_b32 s29, s29, 0xffff\n\t"
    "s_add_u32 s39, s39, 128\n\t"
    "s_cmp_eq_u32 s39, 1152\n\t"
    "s_cselect_b32 s39, 0, s39\n\t"
    "s_add_u32 m0, s37, s28\n\t"
    "s_nop 0\n\t"
    "buffer_load_dwordx4 v6, s[4:7], s38 offen lds\n\t"
    "buffer_load_dwordx4 v7, s[4:7], s38 offen offset:1024 lds\n\t"
    "buffer_load_dwordx4 v8, s[4:7], s38 offen offset:2048 lds\n\t"
    "s_add_u32 s38, s38, 128\n\t"
    "s_cmp_eq_u32 s38, 1152\n\t"
    "s_cselect_b32 s38, 0, s38\n\t"
    "s_add_u32 s28, s28, 12288\n\t"
    "s_cmp_eq_u32 s28, 36864\n\t"
    "s_cselect_b32 s28, 0, s28\n\t"
    "s_waitcnt vmcnt(14)\n\t"
    "s_barrier\n\t"
    "s_lshl_b32 s30, s39, 12\n\t"
    "s_add_u32 m0, s36, s29\n\t"
    "s_nop 0\n\t"
    "buffer_load_dwordx4 v2, s[8:11], s30 offen lds\n\t"
    "buffer_load_dwordx4 v3, s[8:11], s30 offen offset:1024 lds\n\t"
    "buffer_load_dwordx4 v4, s[8:11], s30 offen offset:2048 lds\n\t"
    "buffer_load_dwordx4 v5, s[8:11], s30 offen offset:3072 lds\n\t"
    "s_add_u32 s29, s29, 16384\n\t"
    "s_and_b32 s29, s29, 0xffff\n\t"
    "s_waitcnt vmcnt(11)\n\t"
    "s_barrier\n\t"
    "s_lshl_b32 s30, s39, 12\n\t"
    "s_add_u32 s30, s30, 128\n\t"
    "s_add_u32 m0, s36, s29\n\t"
    "s_nop 0\n\t"
    "buffer_load_dwordx4 v2, s[8:11], s30 offen lds\n\t"
    "buffer_load_dwordx4 v3, s[8:11], s30 offen offset:1024 lds\n\t"
    "buffer_load_dwordx4 v4, s[8:11], s30 offen offset:2048 lds\n\t"
    "buffer_load_dwordx4 v5, s[8:11], s30 offen offset:3072 lds\n\t"
    "s_add_u32 s29, s29, 16384\n\t"
    "s_and_b32 s29, s29, 0xffff\n\t"
    "s_add_u32 s39, s39, 128\n\t"
    "s_cmp_eq_u32 s39, 1152\n\t"
    "s_cselect_b32 s39, 0, s39\n\t"
    "s_waitcnt vmcnt(11)\n\t"
    "s_barrier\n\t"
    "s_add_u32 m0, s36, s65\n\t"
    "s_nop 0\n\t"
    "buffer_load_dwordx4 v9, s[12:15], s68 offen nt lds\n\t"
    "buffer_load_dwordx4 v10, s[12:15], s68 offen offset:1024 nt lds\n\t"
    "buffer_load_dwordx4 v11, s[12:15], s68 offen offset:2048 nt lds\n\t"
    "buffer_load_dwordx4 v12, s[12:15], s68 offen offset:3072 nt lds\n\t"
    "s_add_u32 s68, s68, 8192\n\t"
    "s_add_u32 s65, s65, 16384\n\t"
    "s_and_b32 s65, s65, 0x1ffff\n\t"
    "s_waitcnt vmcnt(8)\n\t"
    "s_barrier\n\t"
    "s_add_u32 m0, s36, s65\n\t"
    "s_nop 0\n\t"
    "buffer_load_dwordx4 v9, s[12:15], s68 offen nt lds\n\t"
    "buffer_load_dwordx4 v10, s[12:15], s68 offen offset:1024 nt lds\n\t"
    "buffer_load_dwordx4 v11, s[12:15], s68 offen offset:2048 nt lds\n\t"
    "buffer_load_dwordx4 v12, s[12:15], s68 offen offset:3072 nt lds\n\t"
    "s_add_u32 s68, s68, 8192\n\t"
    "s_add_u32 s65, s65, 16384\n\t"
    "s_and_b32 s65, s65, 0x1ffff\n\t"
    "s_waitcnt vmcnt(8)\n\t"
    "s_barrier\n\t"
    "s_add_u32 m0, s36, s65\n\t"
    "s_nop 0\n\t"
    "buffer_load_dwordx4 v9, s[12:15], s68 offen nt lds\n\t"
    "buffer_load_dwordx4 v10, s[12:15], s68 offen offset:1024 nt lds\n\t"
    "buffer_load_dwordx4 v11, s[12:15], s68 offen offset:2048 nt lds\n\t"
    "buffer_load_dwordx4 v12, s[12:15], s68 offen offset:3072 nt lds\n\t"
    "s_add_u32 s68, s68, 8192\n\t"
    "s_add_u32 s65, s65, 16384\n\t"
    "s_and_b32 s65, s65, 0x1ffff\n\t"
    "s_barrier\n\t"
    "s_waitcnt lgkmcnt(0)\n\t"
    "s_add_u32 s34, s32, 1\n\t"
    "s_lshl_b32 s32, s34, 4\n\t"
    "s_xor_b32 s33, s33, 0x9e3779b9\n\t"
    "s_lshl_b32 s64, s50, 2\n\t"
    "s_add_u32 s64, s64, s55\n\t"
    "s_lshl_b32 s64, s64, 7\n\t"
    "s_add_u32 s64, s64, 270336\n\t"
    "v_cmp_eq_u32 vcc, 0, v1\n\t"
    "v_mov_b32 v32, s64\n\t"
    "v_bfrev_b32 v29, -2\n\t"
    "v_cndmask_b32 v29, v29, v32, vcc\n\t"
    "s_lshl_b32 s63, s34, 8\n\t"
    "v_mov_b32 v32, s63\n\t"
    "v_mov_b32 v33, s33\n\t"
    "v_mov_b32 v34, s63\n\t"
    "v_mov_b32 v35, s33\n\t"
    "s_cmp_eq_u32 s51, 0\n\t"
    "s_cselect_b32 s45, 0, 0x40000000\n\t"
    "s_mov_b64 exec, 1\n\t"
    "buffer_store_dwordx4 v[32:35], v29, s[24:27], s45 offen offset:16 sc1\n\t"
    "s_mov_b64 exec, -1\n\t"
    "s_add_u32 m0, s36, s65\n\t"
    "s_nop 0\n\t"
    "buffer_load_dwordx4 v9, s[12:15], s68 offen nt lds\n\t"
    "buffer_load_dwordx4 v10, s[12:15], s68 offen offset:1024 nt lds\n\t"
    "s_barrier\n\t"
    "gqa_latt:\n\t"
    "s_mov_b32 s31, s55\n\t"
    "gqa_s104:\n\t"
    "s_sub_u32 s31, s31, 1\n\t"
    "s_cbranch_scc1 gqa_s104e\n\t"
    "s_sleep 1\n\t"
    "s_branch gqa_s104\n\t"
    "gqa_s104e:\n\t"
    "s_add_u32 m0, s36, s65\n\t"
    "s_nop 0\n\t"
    "buffer_load_dwordx4 v11, s[12:15], s68 offen offset:2048 nt lds\n\t"
    "s_sleep 4\n\t"
    "buffer_load_dwordx4 v12, s[12:15], s68 offen offset:3072 nt lds\n\t"
    "s_add_u32 s68, s68, 8192\n\t"
    "s_add_u32 s65, s65, 16384\n\t"
    "s_and_b32 s65, s65, 0x1ffff\n\t"
    "s_sleep 4\n\t"
    "s_add_u32 m0, s36, s65\n\t"
    "s_nop 0\n\t"
    "buffer_load_dwordx4 v9, s[12:15], s68 offen nt lds\n\t"
    "s_barrier\n\t"
    "s_mov_b32 s31, s55\n\t"
    "gqa_s105:\n\t"
    "s_sub_u32 s31, s31, 1\n\t"
    "s_cbranch_scc1 gqa_s105e\n\t"
    "s_sleep 1\n\t"
    "s_branch gqa_s105\n\t"
    "gqa_s105e:\n\t"
    "s_add_u32 m0, s36, s65\n\t"
    "s_nop 0\n\t"
    "buffer_load_dwordx4 v10, s[12:15], s68 offen offset:1024 nt lds\n\t"
    "s_sleep 4\n\t"
    "buffer_load_dwordx4 v11, s[12:15], s68 offen offset:2048 nt lds\n\t"
    "s_sleep 4\n\t"
    "buffer_load_dwordx4 v12, s[12:15], s68 offen offset:3072 nt lds\n\t"
    "s_add_u32 s68, s68, 8192\n\t"
    "s_add_u32 s65, s65, 16384\n\t"
    "s_and_b32 s65, s65, 0x1ffff\n\t"
    "s_waitcnt vmcnt(17)\n\t"
    "s_barrier\n\t"
    "s_mov_b32 s31, s55\n\t"
    "gqa_s106:\n\t"
    "s_sub_u32 s31, s31, 1\n\t"
    "s_cbranch_scc1 gqa_s106e\n\t"
    "s_sleep 1\n\t"
    "s_branch gqa_s106\n\t"
    "gqa_s106e:\n\t"
    "s_add_u32 m0, s36, s65\n\t"
    "s_nop 0\n\t"
    "buffer_load_dwordx4 v9, s[12:15], s68 offen nt lds\n\t"
    "s_sleep 4\n\t"
    "buffer_load_dwordx4 v10, s[12:15], s68 offen offset:1024 nt lds\n\t"
    "s_sleep 4\n\t"
    "buffer_load_dwordx4 v11, s[12:15], s68 offen offset:2048 nt lds\n\t"
    "s_waitcnt vmcnt(16)\n\t"
    "s_barrier\n\t"
    "s_mov_b32 s31, s55\n\t"
    "gqa_s107:\n\t"
    "s_sub_u32 s31, s31, 1\n\t"
    "s_cbranch_scc1 gqa_s107e\n\t"
    "s_sleep 1\n\t"
    "s_branch gqa_s107\n\t"
    "gqa_s107e:\n\t"
    "s_add_u32 m0, s36, s65\n\t"
    "s_nop 0\n\t"
    "buffer_load_dwordx4 v12, s[12:15], s68 offen offset:3072 nt lds\n\t"
    "s_add_u32 s68, s68, 8192\n\t"
    "s_add_u32 s65, s65, 16384\n\t"
    "s_and_b32 s65, s65, 0x1ffff\n\t"
    "s_sleep 4\n\t"
    "s_add_u32 m0, s36, s65\n\t"
    "s_nop 0\n\t"
    "buffer_load_dwordx4 v9, s[12:15], s68 offen nt lds\n\t"
    "s_sleep 4\n\t"
    "buffer_load_dwordx4 v10, s[12:15], s68 offen offset:1024 nt lds\n\t"
    "s_waitcnt vmcnt(15)\n\t"
    "s_barrier\n\t"
    "s_mov_b32 s31, s55\n\t"
    "gqa_s108:\n\t"
    "s_sub_u32 s31, s31, 1\n\t"
    "s_cbranch_scc1 gqa_s108e\n\t"
    "s_sleep 1\n\t"
    "s_branch gqa_s108\n\t"
    "gqa_s108e:\n\t"
    "s_add_u32 m0, s36, s65\n\t"
    "s_nop 0\n\t"
    "buffer_load_dwordx4 v11, s[12:15], s68 offen offset:2048 nt lds\n\t"
    "s_sleep 4\n\t"
    "buffer_load_dwordx4 v12, s[12:15], s68 offen offset:3072 nt lds\n\t"
    "s_add_u32 s68, s68, 8192\n\t"
    "s_add_u32 s65, s65, 16384\n\t"
    "s_and_b32 s65, s65, 0x1ffff\n\t"
    "s_sleep 4\n\t"
    "s_add_u32 m0, s36, s65\n\t"
    "s_nop 0\n\t"
    "buffer_load_dwordx4 v9, s[12:15], s68 offen nt lds\n\t"
    "s_waitcnt vmcnt(13)\n\t"
    "s_barrier\n\t"
    "s_mov_b32 s31, s55\n\t"
    "gqa_s109:\n\t"
    "s_sub_u32 s31, s31, 1\n\t"
    "s_cbranch_scc1 gqa_s109e\n\t"
    "s_sleep 1\n\t"
    "s_branch gqa_s109\n\t"
    "gqa_s109e:\n\t"
    "s_add_u32 m0, s36, s65\n\t"
    "s_nop 0\n\t"
    "buffer_load_dwordx4 v10, s[12:15], s68 offen offset:1024 nt lds\n\t"
    "s_sleep 4\n\t"
    "buffer_load_dwordx4 v11, s[12:15], s68 offen offset:2048 nt lds\n\t"
    "s_sleep 4\n\t"
    "buffer_load_dwordx4 v12, s[12:15], s68 offen offset:3072 nt lds\n\t"
    "s_add_u32 s68, s68, 8192\n\t"
    "s_add_u32 s65, s65, 16384\n\t"
    "s_and_b32 s65, s65, 0x1ffff\n\t"
    "s_waitcnt vmcnt(12)\n\t"
    "s_barrier\n\t"
    "s_mov_b64 exec, 1\n\t"
    "buffer_store_dwordx4 v[32:35], v29, s[24:27], s45 offen sc1\n\t"
    "s_mov_b64 exec, -1\n\t"
    "s_mov_b32 s45, 0x40000000\n\t"
    "s_mov_b32 s31, s55\n\t"
    "gqa_s110:\n\t"
    "s_sub_u32 s31, s31, 1\n\t"
    "s_cbranch_scc1 gqa_s110e\n\t"
    "s_sleep 1\n\t"
    "s_branch gqa_s110\n\t"
    "gqa_s110e:\n\t"
    "s_add_u32 m0, s36, s65\n\t"
    "s_nop 0\n\t"
    "buffer_load_dwordx4 v9, s[12:15], s68 offen nt lds\n\t"
    "s_sleep 4\n\t"
    "buffer_load_dwordx4 v10, s[12:15], s68 offen offset:1024 nt lds\n\t"
    "s_sleep 4\n\t"
    "buffer_load_dwordx4 v11, s[12:15], s68 offen offset:2048 nt lds\n\t"
    "s_waitcnt vmcnt(12)\n\t"
    "s_barrier\n\t"
    "s_mov_b32 s31, s55\n\t"
    "gqa_s111:\n\t"
    "s_sub_u32 s31, s31, 1\n\t"
    "s_cbranch_scc1 gqa_s111e\n\t"
    "s_sleep 1\n\t"
    "s_branch gqa_s111\n\t"
    "gqa_s111e:\n\t"
    "s_add_u32 m0, s36, s65\n\t"
    "s_nop 0\n\t"
    "buffer_load_dwordx4 v12, s[12:15], s68 offen offset:3072 nt lds\n\t"
    "s_add_u32 s68, s68, 8192\n\t"
    "s_add_u32 s65, s65, 16384\n\t"
    "s_and_b32 s65, s65, 0x1ffff\n\t"
    "s_sleep 4\n\t"
    "s_add_u32 m0, s36, s65\n\t"
    "s_nop 0\n\t"
    "buffer_load_dwordx4 v9, s[12:15], s68 offen nt lds\n\t"
    "s_sleep 4\n\t"
    "buffer_load_dwordx4 v10, s[12:15], s68 offen offset:1024 nt lds\n\t"
    "s_waitcnt vmcnt(11)\n\t"
    "s_barrier\n\t"
    "s_mov_b32 s31, s55\n\t"
    "gqa_s112:\n\t"
    "s_sub_u32 s31, s31, 1\n\t"
    "s_cbranch_scc1 gqa_s112e\n\t"
    "s_sleep 1\n\t"
    "s_branch gqa_s112\n\t"
    "gqa_s112e:\n\t"
    "s_add_u32 m0, s36, s65\n\t"
    "s_nop 0\n\t"
    "buffer_load_dwordx4 v11, s[12:15], s68 offen offset:2048 nt lds\n\t"
    "s_sleep 4\n\t"
    "buffer_load_dwordx4 v12, s[12:15], s68 offen offset:3072 nt lds\n\t"
    "s_add_u32 s68, s68, 8192\n\t"
    "s_add_u32 s65, s65, 16384\n\t"
    "s_and_b32 s65, s65, 0x1ffff\n\t"
    "s_sleep 4\n\t"
    "s_add_u32 m0, s36, s65\n\t"
    "s_nop 0\n\t"
    "buffer_load_dwordx4 v9, s[12:15], s68 offen nt lds\n\t"
    "s_waitcnt vmcnt(10)\n\t"
    "s_barrier\n\t"
    "s_mov_b32 s31, s55\n\t"
    "gqa_s113:\n\t"
    "s_sub_u32 s31, s31, 1\n\t"
    "s_cbranch_scc1 gqa_s113e\n\t"
    "s_sleep 1\n\t"
    "s_branch gqa_s113\n\t"
    "gqa_s113e:\n\t"
    "s_add_u32 m0, s36, s65\n\t"
    "s_nop 0\n\t"
    "buffer_load_dwordx4 v10, s[12:15], s68 offen offset:1024 nt lds\n\t"
    "s_sleep 4\n\t"
    "buffer_load_dwordx4 v11, s[12:15], s68 offen offset:2048 nt lds\n\t"
    "s_sleep 4\n\t"
    "buffer_load_dwordx4 v12, s[12:15], s68 offen offset:3072 nt lds\n\t"
    "s_add_u32 s68, s68, 8192\n\t"
    "s_add_u32 s65, s65, 16384\n\t"
    "s_and_b32 s65, s65, 0x1ffff\n\t"
    "s_waitcnt vmcnt(8)\n\t"
    "s_barrier\n\t"
    "s_mov_b32 s31, s55\n\t"
    "gqa_s114:\n\t"
    "s_sub_u32 s31, s31, 1\n\t"
    "s_cbranch_scc1 gqa_s114e\n\t"
    "s_sleep 1\n\t"
    "s_branch gqa_s114\n\t"
    "gqa_s114e:\n\t"
    "s_add_u32 m0, s36, s65\n\t"
    "s_nop 0\n\t"
    "buffer_load_dwordx4 v9, s[12:15], s68 offen nt lds\n\t"
    "s_sleep 4\n\t"
    "buffer_load_dwordx4 v10, s[12:15], s68 offen offset:1024 nt lds\n\t"
    "s_sleep 4\n\t"
    "buffer_load_dwordx4 v11, s[12:15], s68 offen offset:2048 nt lds\n\t"
    "s_waitcnt vmcnt(7)\n\t"
    "s_barrier\n\t"
    "s_mov_b32 s31, s55\n\t"
    "gqa_s115:\n\t"
    "s_sub_u32 s31, s31, 1\n\t"
    "s_cbranch_scc1 gqa_s115e\n\t"
    "s_sleep 1\n\t"
    "s_branch gqa_s115\n\t"
    "gqa_s115e:\n\t"
    "s_add_u32 m0, s36, s65\n\t"
    "s_nop 0\n\t"
    "buffer_load_dwordx4 v12, s[12:15], s68 offen offset:3072 nt lds\n\t"
    "s_add_u32 s68, s68, 8192\n\t"
    "s_add_u32 s65, s65, 16384\n\t"
    "s_and_b32 s65, s65, 0x1ffff\n\t"
    "s_sleep 4\n\t"
    "s_add_u32 m0, s36, s65\n\t"
    "s_nop 0\n\t"
    "buffer_load_dwordx4 v9, s[12:15], s68 offen nt lds\n\t"
    "s_sleep 4\n\t"
    "buffer_load_dwordx4 v10, s[12:15], s68 offen offset:1024 nt lds\n\t"
    "s_waitcnt vmcnt(6)\n\t"
    "s_barrier\n\t"
    "s_mov_b32 s31, s55\n\t"
    "gqa_s116:\n\t"
    "s_sub_u32 s31, s31, 1\n\t"
    "s_cbranch_scc1 gqa_s116e\n\t"
    "s_sleep 1\n\t"
    "s_branch gqa_s116\n\t"
    "gqa_s116e:\n\t"
    "s_add_u32 m0, s36, s65\n\t"
    "s_nop 0\n\t"
    "buffer_load_dwordx4 v11, s[12:15], s68 offen offset:2048 nt lds\n\t"
    "s_sleep 4\n\t"
    "buffer_load_dwordx4 v12, s[12:15], s68 offen offset:3072 nt lds\n\t"
    "s_add_u32 s68, s68, 8192\n\t"
    "s_add_u32 s65, s65, 16384\n\t"
    "s_and_b32 s65, s65, 0x1ffff\n\t"
    "s_mov_b32 s30, s35\n\t"
    "s_add_u32 m0, s36, 65536\n\t"
    "s_nop 0\n\t"
    "buffer_load_dwordx4 v13, s[16:19], s30 offen lds\n\t"
    "buffer_load_dwordx4 v14, s[16:19], s30 offen offset:1024 lds\n\t"
    "buffer_load_dwordx4 v15, s[16:19], s30 offen offset:2048 lds\n\t"
    "buffer_load_dwordx4 v16, s[16:19], s30 offen offset:3072 lds\n\t"
    "s_add_u32 s30, s35, 147456\n\t"
    "s_add_u32 m0, s36, 81920\n\t"
    "s_nop 0\n\t"
    "buffer_load_dwordx4 v13, s[16:19], s30 offen lds\n\t"
    "buffer_load_dwordx4 v14, s[16:19], s30 offen offset:1024 lds\n\t"
    "buffer_load_dwordx4 v15, s[16:19], s30 offen offset:2048 lds\n\t"
    "buffer_load_dwordx4 v16, s[16:19], s30 offen offset:3072 lds\n\t"
    "s_add_u32 s35, s35, 128\n\t"
    "s_cmp_eq_u32 s35, 1152\n\t"
    "s_cselect_b32 s35, 0, s35\n\t"
    "s_waitcnt vmcnt(12)\n\t"
    "s_barrier\n\t"
    "v_mov_b32 v62, 0\n\t"
    "v_mov_b32 v63, 0\n\t"
    "s_mov_b64 exec, 1\n\t"
    "buffer_atomic_or_x2 v[62:63], v29, s[24:27], 0 offen sc0 sc1\n\t"
    "s_mov_b64 exec, -1\n\t"
    "s_mov_b32 s30, s35\n\t"
    "s_add_u32 m0, s36, 98304\n\t"
    "s_nop 0\n\t"
    "buffer_load_dwordx4 v13, s[16:19], s30 offen lds\n\t"
    "buffer_load_dwordx4 v14, s[16:19], s30 offen offset:1024 lds\n\t"
    "buffer_load_dwordx4 v15, s[16:19], s30 offen offset:2048 lds\n\t"
    "buffer_load_dwordx4 v16, s[16:19], s30 offen offset:3072 lds\n\t"
    "s_add_u32 s30, s35, 147456\n\t"
    "s_add_u32 m0, s36, 114688\n\t"
    "s_nop 0\n\t"
    "buffer_load_dwordx4 v13, s[16:19], s30 offen lds\n\t"
    "buffer_load_dwordx4 v14, s[16:19], s30 offen offset:1024 lds\n\t"
    "buffer_load_dwordx4 v15, s[16:19], s30 offen offset:2048 lds\n\t"
    "buffer_load_dwordx4 v16, s[16:19], s30 offen offset:3072 lds\n\t"
    "s_add_u32 s35, s35, 128\n\t"
    "s_cmp_eq_u32 s35, 1152\n\t"
    "s_cselect_b32 s35, 0, s35\n\t"
    "s_waitcnt vmcnt(17)\n\t"
    "s_barrier\n\t"
    "s_mov_b32 s30, s35\n\t"
    "s_add_u32 m0, s36, 0\n\t"
    "s_nop 0\n\t"
    "buffer_load_dwordx4 v13, s[16:19], s30 offen lds\n\t"
    "buffer_load_dwordx4 v14, s[16:19], s30 offen offset:1024 lds\n\t"
    "buffer_load_dwordx4 v15, s[16:19], s30 offen offset:2048 lds\n\t"
    "buffer_load_dwordx4 v16, s[16:19], s30 offen offset:3072 lds\n\t"
    "s_waitcnt vmcnt(4)\n\t"
    "s_barrier\n\t"
    "s_add_u32 s30, s35, 147456\n\t"
    "s_add_u32 m0, s36, 16384\n\t"
    "s_nop 0\n\t"
    "buffer_load_dwordx4 v13, s[16:19], s30 offen lds\n\t"
    "buffer_load_dwordx4 v14, s[16:19], s30 offen offset:1024 lds\n\t"
    "buffer_load_dwordx4 v15, s[16:19], s30 offen offset:2048 lds\n\t"
    "buffer_load_dwordx4 v16, s[16:19], s30 offen offset:3072 lds\n\t"
    "s_add_u32 s35, s35, 128\n\t"
    "s_cmp_eq_u32 s35, 1152\n\t"
    "s_cselect_b32 s35, 0, s35\n\t"
    "s_add_u32 s30, s67, 147456\n\t"
    "buffer_load_dwordx4 v[80:83], v13, s[16:19], s30 offen\n\t"
    "buffer_load_dwordx4 v[84:87], v14, s[16:19], s30 offen offset:1024\n\t"
    "buffer_load_dwordx4 v[88:91], v15, s[16:19], s30 offen offset:2048\n\t"
    "buffer_load_dwordx4 v[92:95], v16, s[16:19], s30 offen offset:3072\n\t"
    "s_add_u32 s67, s67, 128\n\t"
    "s_cmp_eq_u32 s67, 1152\n\t"
    "s_cselect_b32 s67, 0, s67\n\t"
    "s_add_u32 s30, s67, 147456\n\t"
    "buffer_load_dwordx4 v[120:123], v15, s[16:19], s30 offen offset:2048\n\t"
    "buffer_load_dwordx4 v[124:127], v16, s[16:19], s30 offen offset:3072\n\t"
    "s_add_u32 s67, s67, 128\n\t"
    "s_cmp_eq_u32 s67, 1152\n\t"
    "s_cselect_b32 s67, 0, s67\n\t"
    "s_waitcnt vmcnt(6)\n\t"
    "s_barrier\n\t"
    "v_mov_b32 v28, 155648\n\t"
    "ds_read_b128 v[32:35], v28\n\t"
    "s_waitcnt lgkmcnt(0)\n\t"
    "v_or3_b32 v32, v32, v33, v34\n\t"
    "v_or_b32 v32, v32, v35\n\t"
    "s_nop 0\n\t"
    "v_readfirstlane_b32 s30, v32\n\t"
    "s_nop 0\n\t"
    "s_cmp_lg_u32 s30, 0\n\t"
    "s_cbranch_scc1 gqa_lfb\n\t"
    "v_mov_b32 v31, s33\n\t"
    "s_add_u32 s30, s67, 147456\n\t"
    "buffer_load_dwordx4 v[152:155], v15, s[16:19], s30 offen offset:2048\n\t"
    "buffer_load_dwordx4 v[156:159], v16, s[16:19], s30 offen offset:3072\n\t"
    "s_add_u32 s67, s67, 128\n\t"
    "s_cmp_eq_u32 s67, 1152\n\t"
    "s_cselect_b32 s67, 0, s67\n\t"
    "s_add_u32 s30, s67, 147456\n\t"
    "buffer_load_dwordx4 v[184:187], v15, s[16:19], s30 offen offset:2048\n\t"
    "buffer_load_dwordx4 v[188:191], v16, s[16:19], s30 offen offset:3072\n\t"
    "s_add_u32 s67, s67, 128\n\t"
    "s_cmp_eq_u32 s67, 1152\n\t"
    "s_cselect_b32 s67, 0, s67\n\t"
    "s_add_u32 s30, s67, 147456\n\t"
    "buffer_load_dwordx4 v[216:219], v15, s[16:19], s30 offen offset:2048\n\t"
    "buffer_load_dwordx4 v[220:223], v16, s[16:19], s30 offen offset:3072\n\t"
    "s_add_u32 s67, s67, 128\n\t"
    "s_cmp_eq_u32 s67, 1152\n\t"
    "s_cselect_b32 s67, 0, s67\n\t"
    "s_add_u32 s30, s67, 147456\n\t"
    "buffer_load_dwordx4 v[248:251], v15, s[16:19], s30 offen offset:2048\n\t"
    "buffer_load_dwordx4 v[252:255], v16, s[16:19], s30 offen offset:3072\n\t"
    "s_add_u32 s67, s67, 128\n\t"
    "s_cmp_eq_u32 s67, 1152\n\t"
    "s_cselect_b32 s67, 0, s67\n\t"
    "s_mov_b32 s62, 0x40000\n\t"
    "13:\n\t"
    "v_readfirstlane_b32 s30, v62\n\t"
    "v_readfirstlane_b32 s31, v63\n\t"
    "s_nop 0\n\t"
    "s_sub_u32 s30, s30, s63\n\t"
    "s_cmp_le_u32 s30, 16\n\t"
    "s_cselect_b32 s30, s33, -1\n\t"
    "s_cselect_b32 s61, s31, 0\n\t"
    "s_cmp_eq_u32 s30, s61\n\t"
    "s_cbranch_scc1 14f\n\t"
    "s_sub_u32 s62, s62, 1\n\t"
    "s_cbranch_scc1 14f\n\t"
    "s_sleep 8\n\t"
    "v_mov_b32 v62, 0\n\t"
    "v_mov_b32 v63, 0\n\t"
    "s_mov_b64 exec, 1\n\t"
    "buffer_atomic_or_x2 v[62:63], v29, s[24:27], 0 offen sc0 sc1\n\t"
    "s_mov_b64 exec, -1\n\t"
    "s_waitcnt vmcnt(0)\n\t"
    "s_branch 13b\n\t"
    "14:\n\t"
    "s_nop 0\n\t"
    "v_add_u32 v60, s36, v17\n\t"
    "v_add_u32 v61, 0x10000, v60\n\t"
    "s_setprio 3\n\t"
    "s_barrier\n\t"
    "s_mov_b32 s30, s35\n\t"
    "s_add_u32 m0, s36, 32768\n\t"
    "s_nop 0\n\t"
    "buffer_load_dwordx4 v13, s[16:19], s30 offen lds\n\t"
    "buffer_load_dwordx4 v14, s[16:19], s30 offen offset:1024 lds\n\t"
    "buffer_load_dwordx4 v15, s[16:19], s30 offen offset:2048 lds\n\t"
    "buffer_load_dwordx4 v16, s[16:19], s30 offen offset:3072 lds\n\t"
    "s_add_u32 s35, s35, 128\n\t"
    "s_cmp_eq_u32 s35, 1152\n\t"
    "s_cselect_b32 s35, 0, s35\n\t"
    "s_waitcnt lgkmcnt(0)\n\t"
    "s_barrier\n\t"
    "ds_read_b128 v[36:39], v18 offset:0\n\t"
    "ds_read_b128 v[40:43], v18 offset:1024\n\t"
    "ds_read_b128 v[44:47], v18 offset:2048\n\t"
    "s_waitcnt lgkmcnt(0)\n\t"
    "s_lshl_b32 s30, s46, 10\n\t"
    "s_add_u32 s30, s30, 0\n\t"
    "s_cmp_lg_u32 s47, 0\n\t"
    "s_cbranch_scc1 gqa_o117\n\t"
    "s_add_u32 s30, s46, 0\n\t"
    "s_lshr_b32 s30, s30, 4\n\t"
    "s_lshl_b32 s30, s30, 10\n\t"
    "s_add_u32 s30, s30, 156672\n\t"
    "v_add_u32 v28, s30, v17\n\t"
    "s_mov_b32 s30, 0x40000000\n\t"
    "ds_write_b128 v28, v[36:39]\n\t"
    "gqa_o117:\n\t"
    "buffer_store_dwordx4 v[36:39], v17, s[20:23], s30 offen sc1\n\t"
    "s_lshl_b32 s30, s46, 10\n\t"
    "s_add_u32 s30, s30, 1024\n\t"
    "s_cmp_lg_u32 s47, 15\n\t"
    "s_cbranch_scc1 gqa_o118\n\t"
    "s_add_u32 s30, s46, 1\n\t"
    "s_lshr_b32 s30, s30, 4\n\t"
    "s_lshl_b32 s30, s30, 10\n\t"
    "s_add_u32 s30, s30, 156672\n\t"
    "v_add_u32 v28, s30, v17\n\t"
    "s_mov_b32 s30, 0x40000000\n\t"
    "ds_write_b128 v28, v[40:43]\n\t"
    "gqa_o118:\n\t"
    "buffer_store_dwordx4 v[40:43], v17, s[20:23], s30 offen sc1\n\t"
    "s_lshl_b32 s30, s46, 10\n\t"
    "s_add_u32 s30, s30, 2048\n\t"
    "s_cmp_lg_u32 s47, 14\n\t"
    "s_cbranch_scc1 gqa_o119\n\t"
    "s_add_u32 s30, s46, 2\n\t"
    "s_lshr_b32 s30, s30, 4\n\t"
    "s_lshl_b32 s30, s30, 10\n\t"
    "s_add_u32 s30, s30, 156672\n\t"
    "v_add_u32 v28, s30, v17\n\t"
    "s_mov_b32 s30, 0x40000000\n\t"
    "ds_write_b128 v28, v[44:47]\n\t"
    "gqa_o119:\n\t"
    "buffer_store_dwordx4 v[44:47], v17, s[20:23], s30 offen sc1\n\t"
    "s_waitcnt vmcnt(17)\n\t"
    "ds_write_b128 v60, v[80:83] offset:49152\n\t"
    "ds_write_b128 v60, v[84:87] offset:50176\n\t"
    "ds_write_b128 v60, v[88:91] offset:51200\n\t"
    "ds_write_b128 v60, v[92:95] offset:52224\n\t"
    "s_mov_b32 s30, s35\n\t"
    "s_add_u32 m0, s36, 65536\n\t"
    "s_nop 0\n\t"
    "buffer_load_dwordx4 v13, s[16:19], s30 offen lds\n\t"
    "buffer_load_dwordx4 v14, s[16:19], s30 offen offset:1024 lds\n\t"
    "buffer_load_dwordx4 v15, s[16:19], s30 offen offset:2048 lds\n\t"
    "buffer_load_dwordx4 v16, s[16:19], s30 offen offset:3072 lds\n\t"
    "s_add_u32 s30, s35, 147456\n\t"
    "s_add_u32 m0, s36, 81920\n\t"
    "s_nop 0\n\t"
    "buffer_load_dwordx4 v13, s[16:19], s30 offen lds\n\t"
    "buffer_load_dwordx4 v14, s[16:19], s30 offen offset:1024 lds\n\t"
    "s_add_u32 s35, s35, 128\n\t"
    "s_cmp_eq_u32 s35, 1152\n\t"
    "s_cselect_b32 s35, 0, s35\n\t"
    "s_waitcnt lgkmcnt(0)\n\t"
    "s_waitcnt vmcnt(9)\n\t"
    "s_barrier\n\t"
    "ds_read_b128 v[48:51], v18 offset:12288\n\t"
    "ds_read_b128 v[52:55], v18 offset:13312\n\t"
    "ds_read_b128 v[56:59], v18 offset:14336\n\t"
    "s_waitcnt lgkmcnt(0)\n\t"
    "s_lshl_b32 s30, s46, 10\n\t"
    "s_add_u32 s30, s30, 12288\n\t"
    "s_cmp_lg_u32 s47, 4\n\t"
    "s_cbranch_scc1 gqa_o120\n\t"
    "s_add_u32 s30, s46, 12\n\t"
    "s_lshr_b32 s30, s30, 4\n\t"
    "s_lshl_b32 s30, s30, 10\n\t"
    "s_add_u32 s30, s30, 156672\n\t"
    "v_add_u32 v28, s30, v17\n\t"
    "s_mov_b32 s30, 0x40000000\n\t"
    "ds_write_b128 v28, v[48:51]\n\t"
    "gqa_o120:\n\t"
    "buffer_store_dwordx4 v[48:51], v17, s[20:23], s30 offen sc1\n\t"
    "s_lshl_b32 s30, s46, 10\n\t"
    "s_add_u32 s30, s30, 13312\n\t"
    "s_cmp_lg_u32 s47, 3\n\t"
    "s_cbranch_scc1 gqa_o121\n\t"
    "s_add_u32 s30, s46, 13\n\t"
    "s_lshr_b32 s30, s30, 4\n\t"
    "s_lshl_b32 s30, s30, 10\n\t"
    "s_add_u32 s30, s30, 156672\n\t"
    "v_add_u32 v28, s30, v17\n\t"
    "s_mov_b32 s30, 0x40000000\n\t"
    "ds_write_b128 v28, v[52:55]\n\t"
    "gqa_o121:\n\t"
    "buffer_store_dwordx4 v[52:55], v17, s[20:23], s30 offen sc1\n\t"
    "s_lshl_b32 s30, s46, 10\n\t"
    "s_add_u32 s30, s30, 14336\n\t"
    "s_cmp_lg_u32 s47, 2\n\t"
    "s_cbranch_scc1 gqa_o122\n\t"
    "s_add_u32 s30, s46, 14\n\t"
    "s_lshr_b32 s30, s30, 4\n\t"
    "s_lshl_b32 s30, s30, 10\n\t"
    "s_add_u32 s30, s30, 156672\n\t"
    "v_add_u32 v28, s30, v17\n\t"
    "s_mov_b32 s30, 0x40000000\n\t"
    "ds_write_b128 v28, v[56:59]\n\t"
    "gqa_o122:\n\t"
    "buffer_store_dwordx4 v[56:59], v17, s[20:23], s30 offen sc1\n\t"
    "s_mov_b32 s30, s35\n\t"
    "s_add_u32 m0, s36, 98304\n\t"
    "s_nop 0\n\t"
    "buffer_load_dwordx4 v13, s[16:19], s30 offen lds\n\t"
    "buffer_load_dwordx4 v14, s[16:19], s30 offen offset:1024 lds\n\t"
    "buffer_load_dwordx4 v15, s[16:19], s30 offen offset:2048 lds\n\t"
    "buffer_load_dwordx4 v16, s[16:19], s30 offen offset:3072 lds\n\t"
    "s_add_u32 s30, s35, 147456\n\t"
    "s_add_u32 m0, s36, 114688\n\t"
    "s_nop 0\n\t"
    "buffer_load_dwordx4 v13, s[16:19], s30 offen lds\n\t"
    "buffer_load_dwordx4 v14, s[16:19], s30 offen offset:1024 lds\n\t"
    "s_add_u32 s35, s35, 128\n\t"
    "s_cmp_eq_u32 s35, 1152\n\t"
    "s_cselect_b32 s35, 0, s35\n\t"
    "ds_write_b128 v61, v[120:123] offset:18432\n\t"
    "ds_write_b128 v61, v[124:127] offset:19456\n\t"
    "s_waitcnt lgkmcnt(0)\n\t"
    "s_waitcnt vmcnt(9)\n\t"
    "s_barrier\n\t"
    "ds_read_b128 v[36:39], v18 offset:0\n\t"
    "ds_read_b128 v[40:43], v18 offset:1024\n\t"
    "ds_read_b128 v[44:47], v18 offset:2048\n\t"
    "s_waitcnt lgkmcnt(0)\n\t"
    "s_lshl_b32 s30, s46, 10\n\t"
    "s_add_u32 s30, s30, 24576\n\t"
    "s_cmp_lg_u32 s47, 8\n\t"
    "s_cbranch_scc1 gqa_o123\n\t"
    "s_add_u32 s30, s46, 24\n\t"
    "s_lshr_b32 s30, s30, 4\n\t"
    "s_lshl_b32 s30, s30, 10\n\t"
    "s_add_u32 s30, s30, 156672\n\t"
    "v_add_u32 v28, s30, v17\n\t"
    "s_mov_b32 s30, 0x40000000\n\t"
    "ds_write_b128 v28, v[36:39]\n\t"
    "gqa_o123:\n\t"
    "buffer_store_dwordx4 v[36:39], v17, s[20:23], s30 offen sc1\n\t"
    "s_lshl_b32 s30, s46, 10\n\t"
    "s_add_u32 s30, s30, 25600\n\t"
    "s_cmp_lg_u32 s47, 7\n\t"
    "s_cbranch_scc1 gqa_o124\n\t"
    "s_add_u32 s30, s46, 25\n\t"
    "s_lshr_b32 s30, s30, 4\n\t"
    "s_lshl_b32 s30, s30, 10\n\t"
    "s_add_u32 s30, s30, 156672\n\t"
    "v_add_u32 v28, s30, v17\n\t"
    "s_mov_b32 s30, 0x40000000\n\t"
    "ds_write_b128 v28, v[40:43]\n\t"
    "gqa_o124:\n\t"
    "buffer_store_dwordx4 v[40:43], v17, s[20:23], s30 offen sc1\n\t"
    "s_lshl_b32 s30, s46, 10\n\t"
    "s_add_u32 s30, s30, 26624\n\t"
    "s_cmp_lg_u32 s47, 6\n\t"
    "s_cbranch_scc1 gqa_o125\n\t"
    "s_add_u32 s30, s46, 26\n\t"
    "s_lshr_b32 s30, s30, 4\n\t"
    "s_lshl_b32 s30, s30, 10\n\t"
    "s_add_u32 s30, s30, 156672\n\t"
    "v_add_u32 v28, s30, v17\n\t"
    "s_mov_b32 s30, 0x40000000\n\t"
    "ds_write_b128 v28, v[44:47]\n\t"
    "gqa_o125:\n\t"
    "buffer_store_dwordx4 v[44:47], v17, s[20:23], s30 offen sc1\n\t"
    "s_mov_b32 s30, s35\n\t"
    "s_add_u32 m0, s36, 0\n\t"
    "s_nop 0\n\t"
    "buffer_load_dwordx4 v13, s[16:19], s30 offen lds\n\t"
    "buffer_load_dwordx4 v14, s[16:19], s30 offen offset:1024 lds\n\t"
    "buffer_load_dwordx4 v15, s[16:19], s30 offen offset:2048 lds\n\t"
    "buffer_load_dwordx4 v16, s[16:19], s30 offen offset:3072 lds\n\t"
    "s_add_u32 s30, s35, 147456\n\t"
    "s_add_u32 m0, s36, 16384\n\t"
    "s_nop 0\n\t"
    "buffer_load_dwordx4 v13, s[16:19], s30 offen lds\n\t"
    "buffer_load_dwordx4 v14, s[16:19], s30 offen offset:1024 lds\n\t"
    "s_add_u32 s35, s35, 128\n\t"
    "s_cmp_eq_u32 s35, 1152\n\t"
    "s_cselect_b32 s35, 0, s35\n\t"
    "ds_write_b128 v61, v[152:155] offset:51200\n\t"
    "ds_write_b128 v61, v[156:159] offset:52224\n\t"
    "s_waitcnt lgkmcnt(0)\n\t"
    "s_waitcnt vmcnt(9)\n\t"
    "s_barrier\n\t"
    "ds_read_b128 v[48:51], v18 offset:12288\n\t"
    "ds_read_b128 v[52:55], v18 offset:13312\n\t"
    "ds_read_b128 v[56:59], v18 offset:14336\n\t"
    "s_waitcnt lgkmcnt(0)\n\t"
    "s_lshl_b32 s30, s46, 10\n\t"
    "s_add_u32 s30, s30, 36864\n\t"
    "s_cmp_lg_u32 s47, 12\n\t"
    "s_cbranch_scc1 gqa_o126\n\t"
    "s_add_u32 s30, s46, 36\n\t"
    "s_lshr_b32 s30, s30, 4\n\t"
    "s_lshl_b32 s30, s30, 10\n\t"
    "s_add_u32 s30, s30, 156672\n\t"
    "v_add_u32 v28, s30, v17\n\t"
    "s_mov_b32 s30, 0x40000000\n\t"
    "ds_write_b128 v28, v[48:51]\n\t"
    "gqa_o126:\n\t"
    "buffer_store_dwordx4 v[48:51], v17, s[20:23], s30 offen sc1\n\t"
    "s_lshl_b32 s30, s46, 10\n\t"
    "s_add_u32 s30, s30, 37888\n\t"
    "s_cmp_lg_u32 s47, 11\n\t"
    "s_cbranch_scc1 gqa_o127\n\t"
    "s_add_u32 s30, s46, 37\n\t"
    "s_lshr_b32 s30, s30, 4\n\t"
    "s_lshl_b32 s30, s30, 10\n\t"
    "s_add_u32 s30, s30, 156672\n\t"
    "v_add_u32 v28, s30, v17\n\t"
    "s_mov_b32 s30, 0x40000000\n\t"
    "ds_write_b128 v28, v[52:55]\n\t"
    "gqa_o127:\n\t"
    "buffer_store_dwordx4 v[52:55], v17, s[20:23], s30 offen sc1\n\t"
    "s_lshl_b32 s30, s46, 10\n\t"
    "s_add_u32 s30, s30, 38912\n\t"
    "s_cmp_lg_u32 s47, 10\n\t"
    "s_cbranch_scc1 gqa_o128\n\t"
    "s_add_u32 s30, s46, 38\n\t"
    "s_lshr_b32 s30, s30, 4\n\t"
    "s_lshl_b32 s30, s30, 10\n\t"
    "s_add_u32 s30, s30, 156672\n\t"
    "v_add_u32 v28, s30, v17\n\t"
    "s_mov_b32 s30, 0x40000000\n\t"
    "ds_write_b128 v28, v[56:59]\n\t"
    "gqa_o128:\n\t"
    "buffer_store_dwordx4 v[56:59], v17, s[20:23], s30 offen sc1\n\t"
    "s_mov_b32 s30, s35\n\t"
    "s_add_u32 m0, s36, 32768\n\t"
    "s_nop 0\n\t"
    "buffer_load_dwordx4 v13, s[16:19], s30 offen lds\n\t"
    "buffer_load_dwordx4 v14, s[16:19], s30 offen offset:1024 lds\n\t"
    "buffer_load_dwordx4 v15, s[16:19], s30 offen offset:2048 lds\n\t"
    "buffer_load_dwordx4 v16, s[16:19], s30 offen offset:3072 lds\n\t"
    "s_add_u32 s30, s35, 147456\n\t"
    "s_add_u32 m0, s36, 49152\n\t"
    "s_nop 0\n\t"
    "buffer_load_dwordx4 v13, s[16:19], s30 offen lds\n\t"
    "buffer_load_dwordx4 v14, s[16:19], s30 offen offset:1024 lds\n\t"
    "s_add_u32 s35, s35, 128\n\t"
    "s_cmp_eq_u32 s35, 1152\n\t"
    "s_cselect_b32 s35, 0, s35\n\t"
    "ds_write_b128 v60, v[184:187] offset:18432\n\t"
    "ds_write_b128 v60, v[188:191] offset:19456\n\t"
    "s_waitcnt lgkmcnt(0)\n\t"
    "s_waitcnt vmcnt(9)\n\t"
    "s_barrier\n\t"
    "ds_read_b128 v[36:39], v18 offset:0\n\t"
    "ds_read_b128 v[40:43], v18 offset:1024\n\t"
    "ds_read_b128 v[44:47], v18 offset:2048\n\t"
    "s_waitcnt lgkmcnt(0)\n\t"
    "s_lshl_b32 s30, s46, 10\n\t"
    "s_add_u32 s30, s30, 49152\n\t"
    "s_cmp_lg_u32 s47, 0\n\t"
    "s_cbranch_scc1 gqa_o129\n\t"
    "s_add_u32 s30, s46, 48\n\t"
    "s_lshr_b32 s30, s30, 4\n\t"
    "s_lshl_b32 s30, s30, 10\n\t"
    "s_add_u32 s30, s30, 156672\n\t"
    "v_add_u32 v28, s30, v17\n\t"
    "s_mov_b32 s30, 0x40000000\n\t"
    "ds_write_b128 v28, v[36:39]\n\t"
    "gqa_o129:\n\t"
    "buffer_store_dwordx4 v[36:39], v17, s[20:23], s30 offen sc1\n\t"
    "s_lshl_b32 s30, s46, 10\n\t"
    "s_add_u32 s30, s30, 50176\n\t"
    "s_cmp_lg_u32 s47, 15\n\t"
    "s_cbranch_scc1 gqa_o130\n\t"
    "s_add_u32 s30, s46, 49\n\t"
    "s_lshr_b32 s30, s30, 4\n\t"
    "s_lshl_b32 s30, s30, 10\n\t"
    "s_add_u32 s30, s30, 156672\n\t"
    "v_add_u32 v28, s30, v17\n\t"
    "s_mov_b32 s30, 0x40000000\n\t"
    "ds_write_b128 v28, v[40:43]\n\t"
    "gqa_o130:\n\t"
    "buffer_store_dwordx4 v[40:43], v17, s[20:23], s30 offen sc1\n\t"
    "s_lshl_b32 s30, s46, 10\n\t"
    "s_add_u32 s30, s30, 51200\n\t"
    "s_cmp_lg_u32 s47, 14\n\t"
    "s_cbranch_scc1 gqa_o131\n\t"
    "s_add_u32 s30, s46, 50\n\t"
    "s_lshr_b32 s30, s30, 4\n\t"
    "s_lshl_b32 s30, s30, 10\n\t"
    "s_add_u32 s30, s30, 156672\n\t"
    "v_add_u32 v28, s30, v17\n\t"
    "s_mov_b32 s30, 0x40000000\n\t"
    "ds_write_b128 v28, v[44:47]\n\t"
    "gqa_o131:\n\t"
    "buffer_store_dwordx4 v[44:47], v17, s[20:23], s30 offen sc1\n\t"
    "s_mov_b32 s30, s35\n\t"
    "s_add_u32 m0, s36, 65536\n\t"
    "s_nop 0\n\t"
    "buffer_load_dwordx4 v13, s[16:19], s30 offen lds\n\t"
    "buffer_load_dwordx4 v14, s[16:19], s30 offen offset:1024 lds\n\t"
    "buffer_load_dwordx4 v15, s[16:19], s30 offen offset:2048 lds\n\t"
    "buffer_load_dwordx4 v16, s[16:19], s30 offen offset:3072 lds\n\t"
    "s_add_u32 s30, s35, 147456\n\t"
    "s_add_u32 m0, s36, 81920\n\t"
    "s_nop 0\n\t"
    "buffer_load_dwordx4 v13, s[16:19], s30 offen lds\n\t"
    "buffer_load_dwordx4 v14, s[16:19], s30 offen offset:1024 lds\n\t"
    "s_add_u32 s35, s35, 128\n\t"
    "s_cmp_eq_u32 s35, 1152\n\t"
    "s_cselect_b32 s35, 0, s35\n\t"
    "ds_write_b128 v60, v[216:219] offset:51200\n\t"
    "ds_write_b128 v60, v[220:223] offset:52224\n\t"
    "s_waitcnt lgkmcnt(0)\n\t"
    "s_waitcnt vmcnt(9)\n\t"
    "s_barrier\n\t"
    "ds_read_b128 v[48:51], v18 offset:12288\n\t"
    "ds_read_b128 v[52:55], v18 offset:13312\n\t"
    "ds_read_b128 v[56:59], v18 offset:14336\n\t"
    "s_waitcnt lgkmcnt(0)\n\t"
    "s_lshl_b32 s30, s46, 10\n\t"
    "s_add_u32 s30, s30, 61440\n\t"
    "s_cmp_lg_u32 s47, 4\n\t"
    "s_cbranch_scc1 gqa_o132\n\t"
    "s_add_u32 s30, s46, 60\n\t"
    "s_lshr_b32 s30, s30, 4\n\t"
    "s_lshl_b32 s30, s30, 10\n\t"
    "s_add_u32 s30, s30, 156672\n\t"
    "v_add_u32 v28, s30, v17\n\t"
    "s_mov_b32 s30, 0x40000000\n\t"
    "ds_write_b128 v28, v[48:51]\n\t"
    "gqa_o132:\n\t"
    "buffer_store_dwordx4 v[48:51], v17, s[20:23], s30 offen sc1\n\t"
    "s_lshl_b32 s30, s46, 10\n\t"
    "s_add_u32 s30, s30, 62464\n\t"
    "s_cmp_lg_u32 s47, 3\n\t"
    "s_cbranch_scc1 gqa_o133\n\t"
    "s_add_u32 s30, s46, 61\n\t"
    "s_lshr_b32 s30, s30, 4\n\t"
    "s_lshl_b32 s30, s30, 10\n\t"
    "s_add_u32 s30, s30, 156672\n\t"
    "v_add_u32 v28, s30, v17\n\t"
    "s_mov_b32 s30, 0x40000000\n\t"
    "ds_write_b128 v28, v[52:55]\n\t"
    "gqa_o133:\n\t"
    "buffer_store_dwordx4 v[52:55], v17, s[20:23], s30 offen sc1\n\t"
    "s_lshl_b32 s30, s46, 10\n\t"
    "s_add_u32 s30, s30, 63488\n\t"
    "s_cmp_lg_u32 s47, 2\n\t"
    "s_cbranch_scc1 gqa_o134\n\t"
    "s_add_u32 s30, s46, 62\n\t"
    "s_lshr_b32 s30, s30, 4\n\t"
    "s_lshl_b32 s30, s30, 10\n\t"
    "s_add_u32 s30, s30, 156672\n\t"
    "v_add_u32 v28, s30, v17\n\t"
    "s_mov_b32 s30, 0x40000000\n\t"
    "ds_write_b128 v28, v[56:59]\n\t"
    "gqa_o134:\n\t"
    "buffer_store_dwordx4 v[56:59], v17, s[20:23], s30 offen sc1\n\t"
    "ds_write_b128 v61, v[248:251] offset:18432\n\t"
    "ds_write_b128 v61, v[252:255] offset:19456\n\t"
    "v_mov_b32 v30, 1\n\t"
    "v_mov_b32 v31, 0\n\t"
    "s_mov_b64 exec, 1\n\t"
    "buffer_atomic_add_x2 v[30:31], v29, s[24:27], 0 offen offset:16 sc1\n\t"
    "s_mov_b64 exec, -1\n\t"
    "s_waitcnt lgkmcnt(0)\n\t"
    "s_waitcnt vmcnt(4)\n\t"
    "s_barrier\n\t"
    "ds_read_b128 v[36:39], v18 offset:0\n\t"
    "ds_read_b128 v[40:43], v18 offset:1024\n\t"
    "ds_read_b128 v[44:47], v18 offset:2048\n\t"
    "s_waitcnt lgkmcnt(0)\n\t"
    "s_lshl_b32 s30, s46, 10\n\t"
    "s_add_u32 s30, s30, 73728\n\t"
    "s_cmp_lg_u32 s47, 8\n\t"
    "s_cbranch_scc1 gqa_o135\n\t"
    "s_add_u32 s30, s46, 72\n\t"
    "s_lshr_b32 s30, s30, 4\n\t"
    "s_lshl_b32 s30, s30, 10\n\t"
    "s_add_u32 s30, s30, 156672\n\t"
    "v_add_u32 v28, s30, v17\n\t"
    "s_mov_b32 s30, 0x40000000\n\t"
    "ds_write_b128 v28, v[36:39]\n\t"
    "gqa_o135:\n\t"
    "buffer_store_dwordx4 v[36:39], v17, s[20:23], s30 offen sc1\n\t"
    "s_lshl_b32 s30, s46, 10\n\t"
    "s_add_u32 s30, s30, 74752\n\t"
    "s_cmp_lg_u32 s47, 7\n\t"
    "s_cbranch_scc1 gqa_o136\n\t"
    "s_add_u32 s30, s46, 73\n\t"
    "s_lshr_b32 s30, s30, 4\n\t"
    "s_lshl_b32 s30, s30, 10\n\t"
    "s_add_u32 s30, s30, 156672\n\t"
    "v_add_u32 v28, s30, v17\n\t"
    "s_mov_b32 s30, 0x40000000\n\t"
    "ds_write_b128 v28, v[40:43]\n\t"
    "gqa_o136:\n\t"
    "buffer_store_dwordx4 v[40:43], v17, s[20:23], s30 offen sc1\n\t"
    "s_lshl_b32 s30, s46, 10\n\t"
    "s_add_u32 s30, s30, 75776\n\t"
    "s_cmp_lg_u32 s47, 6\n\t"
    "s_cbranch_scc1 gqa_o137\n\t"
    "s_add_u32 s30, s46, 74\n\t"
    "s_lshr_b32 s30, s30, 4\n\t"
    "s_lshl_b32 s30, s30, 10\n\t"
    "s_add_u32 s30, s30, 156672\n\t"
    "v_add_u32 v28, s30, v17\n\t"
    "s_mov_b32 s30, 0x40000000\n\t"
    "ds_write_b128 v28, v[44:47]\n\t"
    "gqa_o137:\n\t"
    "buffer_store_dwordx4 v[44:47], v17, s[20:23], s30 offen sc1\n\t"
    "s_waitcnt lgkmcnt(0)\n\t"
    "s_barrier\n\t"
    "ds_read_b128 v[48:51], v18 offset:12288\n\t"
    "ds_read_b128 v[52:55], v18 offset:13312\n\t"
    "ds_read_b128 v[56:59], v18 offset:14336\n\t"
    "s_waitcnt lgkmcnt(0)\n\t"
    "s_lshl_b32 s30, s46, 10\n\t"
    "s_add_u32 s30, s30, 86016\n\t"
    "s_cmp_lg_u32 s47, 12\n\t"
    "s_cbranch_scc1 gqa_o138\n\t"
    "s_add_u32 s30, s46, 84\n\t"
    "s_lshr_b32 s30, s30, 4\n\t"
    "s_lshl_b32 s30, s30, 10\n\t"
    "s_add_u32 s30, s30, 156672\n\t"
    "v_add_u32 v28, s30, v17\n\t"
    "s_mov_b32 s30, 0x40000000\n\t"
    "ds_write_b128 v28, v[48:51]\n\t"
    "gqa_o138:\n\t"
    "buffer_store_dwordx4 v[48:51], v17, s[20:23], s30 offen sc1\n\t"
    "s_lshl_b32 s30, s46, 10\n\t"
    "s_add_u32 s30, s30, 87040\n\t"
    "s_cmp_lg_u32 s47, 11\n\t"
    "s_cbranch_scc1 gqa_o139\n\t"
    "s_add_u32 s30, s46, 85\n\t"
    "s_lshr_b32 s30, s30, 4\n\t"
    "s_lshl_b32 s30, s30, 10\n\t"
    "s_add_u32 s30, s30, 156672\n\t"
    "v_add_u32 v28, s30, v17\n\t"
    "s_mov_b32 s30, 0x40000000\n\t"
    "ds_write_b128 v28, v[52:55]\n\t"
    "gqa_o139:\n\t"
    "buffer_store_dwordx4 v[52:55], v17, s[20:23], s30 offen sc1\n\t"
    "s_lshl_b32 s30, s46, 10\n\t"
    "s_add_u32 s30, s30, 88064\n\t"
    "s_cmp_lg_u32 s47, 10\n\t"
    "s_cbranch_scc1 gqa_o140\n\t"
    "s_add_u32 s30, s46, 86\n\t"
    "s_lshr_b32 s30, s30, 4\n\t"
    "s_lshl_b32 s30, s30, 10\n\t"
    "s_add_u32 s30, s30, 156672\n\t"
    "v_add_u32 v28, s30, v17\n\t"
    "s_mov_b32 s30, 0x40000000\n\t"
    "ds_write_b128 v28, v[56:59]\n\t"
    "gqa_o140:\n\t"
    "buffer_store_dwordx4 v[56:59], v17, s[20:23], s30 offen sc1\n\t"
    "s_waitcnt vmcnt(7)\n\t"
    "v_mov_b32 v30, 1\n\t"
    "v_mov_b32 v31, 0\n\t"
    "s_mov_b64 exec, 1\n\t"
    "buffer_atomic_add_x2 v[30:31], v29, s[24:27], 0 offen offset:24 sc1\n\t"
    "s_mov_b64 exec, -1\n\t"
    "s_lshl_b32 s14, s55, 4\n\t"
    "s_add_u32 s14, s14, s51\n\t"
    "s_mul_i32 s15, s14, 43\n\t"
    "s_lshr_b32 s15, s15, 9\n\t"
    "s_mul_i32 s31, s15, 12\n\t"
    "s_sub_u32 s31, s14, s31\n\t"
    "s_mul_i32 s31, s31, 11\n\t"
    "s_lshr_b32 s31, s31, 5\n\t"
    "s_lshl_b32 s30, s50, 2\n\t"
    "s_add_u32 s30, s30, s31\n\t"
    "s_lshl_b32 s30, s30, 7\n\t"
    "s_add_u32 s30, s30, 270336\n\t"
    "s_cmp_lt_u32 s15, 4\n\t"
    "s_cselect_b32 s31, 16, 24\n\t"
    "s_add_u32 s30, s30, s31\n\t"
    "v_mov_b32 v3, 0\n\t"
    "v_mov_b32 v4, 0\n\t"
    "v_mov_b32 v5, 0\n\t"
    "s_mov_b64 exec, 1\n\t"
    "buffer_atomic_or_x2 v[4:5], v3, s[24:27], s30 offen sc0 sc1\n\t"
    "s_mov_b64 exec, -1\n\t"
    "s_waitcnt lgkmcnt(0)\n\t"
    "s_setprio 0\n\t"
    "v_mov_b32 v32, 1\n\t"
    "v_mov_b32 v33, 0\n\t"
    "s_waitcnt vmcnt(0)\n\t"
    "s_mov_b64 exec, 1\n\t"
    "buffer_atomic_add_x2 v[32:33], v29, s[24:27], 0 offen sc1\n\t"
    "s_mov_b64 exec, -1\n\t"
    "s_mov_b32 s45, 0\n\t"
    "v_readfirstlane_b32 s30, v4\n\t"
    "v_readfirstlane_b32 s31, v5\n\t"
    "s_add_u32 s45, s63, 16\n\t"
    "s_xor_b32 s30, s30, s45\n\t"
    "s_xor_b32 s31, s31, s33\n\t"
    "s_or_b32 s30, s30, s31\n\t"
    "s_cselect_b32 s45, 0, 1\n\t"
    "s_mov_b32 s14, s55\n\t"
    "s_cmp_lg_u32 s55, 3\n\t"
    "s_cbranch_scc1 gqa_reduce\n\t"
    "s_setprio 2\n\t"
    "gqa_reduce:\n\t"
    "s_lshl_b32 s14, s14, 4\n\t"
    "s_add_u32 s14, s14, s51\n\t"
    "s_cmp_ge_u32 s14, 108\n\t"
    "s_cbranch_scc1 8f\n\t"
    "v_mbcnt_lo_u32_b32 v1, -1, 0\n\t"
    "v_mbcnt_hi_u32_b32 v1, -1, v1\n\t"
    "s_mul_i32 s15, s14, 43\n\t"
    "s_lshr_b32 s15, s15, 9\n\t"
    "s_mul_i32 s16, s15, 12\n\t"
    "s_sub_u32 s16, s14, s16\n\t"
    "s_mul_i32 s17, s16, 11\n\t"
    "s_lshr_b32 s17, s17, 5\n\t"
    "s_lshl_b32 s19, s50, 2\n\t"
    "s_add_u32 s19, s19, s17\n\t"
    "s_lshl_b32 s19, s19, 7\n\t"
    "s_add_u32 s19, s19, 270336\n\t"
    "s_cmp_lt_u32 s15, 4\n\t"
    "s_cselect_b32 s30, 16, 24\n\t"
    "s_cmp_lt_u32 s15, 6\n\t"
    "s_cselect_b32 s30, s30, 0\n\t"
    "s_cmp_eq_u32 s15, 8\n\t"
    "s_cselect_b32 s30, 8, s30\n\t"
    "s_add_u32 s19, s19, s30\n\t"
    "v_mov_b32 v3, 0\n\t"
    "s_mov_b64 s[62:63], 1\n\t"
    "s_mov_b64 exec, 1\n\t"
    "s_cmp_eq_u32 s45, 1\n\t"
    "s_cbranch_scc1 23f\n\t"
    "v_mov_b32 v6, 0\n\t"
    "v_mov_b32 v7, 0\n\t"
    "buffer_atomic_or_x2 v[6:7], v3, s[24:27], s19 offen sc0 sc1\n\t"
    "23:\n\t"
    "s_mov_b64 exec, -1\n\t"
    "s_mul_i32 s18, s17, 3\n\t"
    "s_sub_u32 s18, s16, s18\n\t"
    "s_add_u32 s20, s32, s15\n\t"
    "s_add_u32 s20, s20, 1\n\t"
    "s_sub_u32 s21, 8, s15\n\t"
    "s_lshl_b32 s30, s34, 8\n\t"
    "s_add_u32 s30, s30, 16\n\t"
    "s_mov_b32 s20, s30\n\t"
    "s_mov_b32 s21, 0\n\t"
    "s_mul_i32 s30, s50, 1769472\n\t"
    "s_add_u32 s4, s56, s30\n\t"
    "s_addc_u32 s5, s57, 0\n\t"
    "s_add_u32 s4, s4, 1048576\n\t"
    "s_addc_u32 s5, s5, 0\n\t"
    "s_and_b32 s5, s5, 0xffff\n\t"
    "s_mov_b32 s6, 1769472\n\t"
    "s_mov_b32 s7, 0x20000\n\t"
    "s_mul_i32 s30, s50, 110592\n\t"
    "s_add_u32 s8, s58, s30\n\t"
    "s_addc_u32 s9, s59, 0\n\t"
    "s_and_b32 s9, s9, 0xffff\n\t"
    "s_mov_b32 s10, 110592\n\t"
    "s_mov_b32 s11, 0x20000\n\t"
    "v_lshlrev_b32 v2, 4, v1\n\t"
    "v_and_b32 v4, 15, v1\n\t"
    "v_mul_u32_u24 v4, 1152, v4\n\t"
    "v_bfe_u32 v5, v1, 4, 1\n\t"
    "v_lshl_add_u32 v4, v5, 5, v4\n\t"
    "v_lshrrev_b32 v5, 5, v1\n\t"
    "v_lshl_add_u32 v4, v5, 4, v4\n\t"
    "s_mov_b32 s12, 0x3f80\n\t"
    "s_mov_b32 s13, 0x3f800000\n\t"
    "s_add_u32 s60, s15, s53\n\t"
    "s_cmp_ge_u32 s60, 9\n\t"
    "s_cselect_b32 s31, 9, 0\n\t"
    "s_sub_u32 s60, s60, s31\n\t"
    "s_lshl_b32 s60, s60, 7\n\t"
    "s_lshr_b32 s31, s17, 1\n\t"
    "s_lshl_b32 s31, s31, 6\n\t"
    "s_add_u32 s60, s60, s31\n\t"
    "s_and_b32 s31, s17, 1\n\t"
    "s_mul_i32 s31, s31, 3\n\t"
    "s_add_u32 s31, s31, s18\n\t"
    "s_mul_i32 s31, s31, 18432\n\t"
    "s_add_u32 s60, s60, s31\n\t"
    "s_lshl_b32 s23, s14, 10\n\t"
    "s_mov_b32 s22, 0x40000\n\t"
    "s_barrier\n\t"
    "s_cmp_eq_u32 s45, 1\n\t"
    "s_cbranch_scc1 6f\n\t"
    "s_waitcnt vmcnt(0)\n\t"
    "4:\n\t"
    "s_mov_b64 exec, s[62:63]\n\t"
    "v_subrev_u32 v6, s20, v6\n\t"
    "v_cmp_ge_u32 vcc, s21, v6\n\t"
    "v_cmp_eq_u32 s[28:29], s33, v7\n\t"
    "s_nop 1\n\t"
    "s_and_b64 vcc, vcc, s[28:29]\n\t"
    "s_cmp_eq_u64 vcc, exec\n\t"
    "s_cbranch_scc1 6f\n\t"
    "v_mov_b32 v20, 0\n\t"
    "v_mov_b32 v21, 0\n\t"
    "buffer_atomic_or_x2 v[20:21], v3, s[24:27], s19 offen sc0 sc1\n\t"
    "s_sleep 6\n\t"
    "v_mov_b32 v22, 0\n\t"
    "v_mov_b32 v23, 0\n\t"
    "buffer_atomic_or_x2 v[22:23], v3, s[24:27], s19 offen sc0 sc1\n\t"
    "s_sleep 6\n\t"
    "17:\n\t"
    "v_mov_b32 v24, 0\n\t"
    "v_mov_b32 v25, 0\n\t"
    "buffer_atomic_or_x2 v[24:25], v3, s[24:27], s19 offen sc0 sc1\n\t"
    "s_waitcnt vmcnt(2)\n\t"
    "v_subrev_u32 v20, s20, v20\n\t"
    "v_cmp_ge_u32 vcc, s21, v20\n\t"
    "v_cmp_eq_u32 s[28:29], s33, v21\n\t"
    "s_nop 1\n\t"
    "s_and_b64 vcc, vcc, s[28:29]\n\t"
    "s_cmp_eq_u64 vcc, exec\n\t"
    "s_cbranch_scc1 6f\n\t"
    "s_sleep 6\n\t"
    "v_mov_b32 v20, 0\n\t"
    "v_mov_b32 v21, 0\n\t"
    "buffer_atomic_or_x2 v[20:21], v3, s[24:27], s19 offen sc0 sc1\n\t"
    "s_waitcnt vmcnt(2)\n\t"
    "v_subrev_u32 v22, s20, v22\n\t"
    "v_cmp_ge_u32 vcc, s21, v22\n\t"
    "v_cmp_eq_u32 s[28:29], s33, v23\n\t"
    "s_nop 1\n\t"
    "s_and_b64 vcc, vcc, s[28:29]\n\t"
    "s_cmp_eq_u64 vcc, exec\n\t"
    "s_cbranch_scc1 6f\n\t"
    "s_sleep 6\n\t"
    "v_mov_b32 v22, 0\n\t"
    "v_mov_b32 v23, 0\n\t"
    "buffer_atomic_or_x2 v[22:23], v3, s[24:27], s19 offen sc0 sc1\n\t"
    "s_waitcnt vmcnt(2)\n\t"
    "v_subrev_u32 v24, s20, v24\n\t"
    "v_cmp_ge_u32 vcc, s21, v24\n\t"
    "v_cmp_eq_u32 s[28:29], s33, v25\n\t"
    "s_nop 1\n\t"
    "s_and_b64 vcc, vcc, s[28:29]\n\t"
    "s_cmp_eq_u64 vcc, exec\n\t"
    "s_cbranch_scc1 6f\n\t"
    "s_sleep 6\n\t"
    "s_sub_u32 s22, s22, 1\n\t"
    "s_cbranch_scc0 17b\n\t"
    "6:\n\t"
    "s_mov_b64 exec, -1\n\t"
    "s_lshr_b32 s30, s14, 4\n\t"
    "s_lshl_b32 s30, s30, 10\n\t"
    "s_add_u32 s30, s30, 156672\n\t"
    "v_add_u32 v5, s30, v2\n\t"
    "ds_read_b128 v[100:103], v5\n\t"
    "s_cmp_eq_u32 s51, 0\n\t"
    "s_cselect_b32 s31, 0x40000000, s23\n\t"
    "buffer_load_dwordx4 v[36:39], v2, s[4:7], s31 offen sc1 nt\n\t"
    "s_add_u32 s23, s23, 110592\n\t"
    "s_cmp_eq_u32 s51, 1\n\t"
    "s_cselect_b32 s31, 0x40000000, s23\n\t"
    "buffer_load_dwordx4 v[40:43], v2, s[4:7], s31 offen sc1 nt\n\t"
    "s_add_u32 s23, s23, 110592\n\t"
    "s_cmp_eq_u32 s51, 2\n\t"
    "s_cselect_b32 s31, 0x40000000, s23\n\t"
    "buffer_load_dwordx4 v[44:47], v2, s[4:7], s31 offen sc1 nt\n\t"
    "s_add_u32 s23, s23, 110592\n\t"
    "s_cmp_eq_u32 s51, 3\n\t"
    "s_cselect_b32 s31, 0x40000000, s23\n\t"
    "buffer_load_dwordx4 v[48:51], v2, s[4:7], s31 offen sc1 nt\n\t"
    "s_add_u32 s23, s23, 110592\n\t"
    "s_cmp_eq_u32 s51, 4\n\t"
    "s_cselect_b32 s31, 0x40000000, s23\n\t"
    "buffer_load_dwordx4 v[52:55], v2, s[4:7], s31 offen sc1 nt\n\t"
    "s_add_u32 s23, s23, 110592\n\t"
    "s_cmp_eq_u32 s51, 5\n\t"
    "s_cselect_b32 s31, 0x40000000, s23\n\t"
    "buffer_load_dwordx4 v[56:59], v2, s[4:7], s31 offen sc1 nt\n\t"
    "s_add_u32 s23, s23, 110592\n\t"
    "s_cmp_eq_u32 s51, 6\n\t"
    "s_cselect_b32 s31, 0x40000000, s23\n\t"
    "buffer_load_dwordx4 v[60:63], v2, s[4:7], s31 offen sc1 nt\n\t"
    "s_add_u32 s23, s23, 110592\n\t"
    "s_cmp_eq_u32 s51, 7\n\t"
    "s_cselect_b32 s31, 0x40000000, s23\n\t"
    "buffer_load_dwordx4 v[64:67], v2, s[4:7], s31 offen sc1 nt\n\t"
    "s_add_u32 s23, s23, 110592\n\t"
    "s_cmp_eq_u32 s51, 8\n\t"
    "s_cselect_b32 s31, 0x40000000, s23\n\t"
    "buffer_load_dwordx4 v[68:71], v2, s[4:7], s31 offen sc1 nt\n\t"
    "s_add_u32 s23, s23, 110592\n\t"
    "s_cmp_eq_u32 s51, 9\n\t"
    "s_cselect_b32 s31, 0x40000000, s23\n\t"
    "buffer_load_dwordx4 v[72:75], v2, s[4:7], s31 offen sc1 nt\n\t"
    "s_add_u32 s23, s23, 110592\n\t"
    "s_cmp_eq_u32 s51, 10\n\t"
    "s_cselect_b32 s31, 0x40000000, s23\n\t"
    "buffer_load_dwordx4 v[76:79], v2, s[4:7], s31 offen sc1 nt\n\t"
    "s_add_u32 s23, s23, 110592\n\t"
    "s_cmp_eq_u32 s51, 11\n\t"
    "s_cselect_b32 s31, 0x40000000, s23\n\t"
    "buffer_load_dwordx4 v[80:83], v2, s[4:7], s31 offen sc1 nt\n\t"
    "s_add_u32 s23, s23, 110592\n\t"
    "s_cmp_eq_u32 s51, 12\n\t"
    "s_cselect_b32 s31, 0x40000000, s23\n\t"
    "buffer_load_dwordx4 v[84:87], v2, s[4:7], s31 offen sc1 nt\n\t"
    "s_add_u32 s23, s23, 110592\n\t"
    "s_cmp_eq_u32 s51, 13\n\t"
    "s_cselect_b32 s31, 0x40000000, s23\n\t"
    "buffer_load_dwordx4 v[88:91], v2, s[4:7], s31 offen sc1 nt\n\t"
    "s_add_u32 s23, s23, 110592\n\t"
    "s_cmp_eq_u32 s51, 14\n\t"
    "s_cselect_b32 s31, 0x40000000, s23\n\t"
    "buffer_load_dwordx4 v[92:95], v2, s[4:7], s31 offen sc1 nt\n\t"
    "s_add_u32 s23, s23, 110592\n\t"
    "s_cmp_eq_u32 s51, 15\n\t"
    "s_cselect_b32 s31, 0x40000000, s23\n\t"
    "buffer_load_dwordx4 v[96:99], v2, s[4:7], s31 offen sc1 nt\n\t"
    "s_waitcnt lgkmcnt(0)\n\t"
    "v_dot2_f32_bf16 v8, v100, s12, 0\n\t"
    "v_dot2_f32_bf16 v9, v100, s13, 0\n\t"
    "v_dot2_f32_bf16 v10, v101, s12, 0\n\t"
    "v_dot2_f32_bf16 v11, v101, s13, 0\n\t"
    "v_dot2_f32_bf16 v12, v102, s12, 0\n\t"
    "v_dot2_f32_bf16 v13, v102, s13, 0\n\t"
    "v_dot2_f32_bf16 v14, v103, s12, 0\n\t"
    "v_dot2_f32_bf16 v15, v103, s13, 0\n\t"
    "s_waitcnt vmcnt(15)\n\t"
    "v_dot2_f32_bf16 v8, v36, s12, v8\n\t"
    "v_dot2_f32_bf16 v9, v36, s13, v9\n\t"
    "v_dot2_f32_bf16 v10, v37, s12, v10\n\t"
    "v_dot2_f32_bf16 v11, v37, s13, v11\n\t"
    "v_dot2_f32_bf16 v12, v38, s12, v12\n\t"
    "v_dot2_f32_bf16 v13, v38, s13, v13\n\t"
    "v_dot2_f32_bf16 v14, v39, s12, v14\n\t"
    "v_dot2_f32_bf16 v15, v39, s13, v15\n\t"
    "s_waitcnt vmcnt(14)\n\t"
    "v_dot2_f32_bf16 v8, v40, s12, v8\n\t"
    "v_dot2_f32_bf16 v9, v40, s13, v9\n\t"
    "v_dot2_f32_bf16 v10, v41, s12, v10\n\t"
    "v_dot2_f32_bf16 v11, v41, s13, v11\n\t"
    "v_dot2_f32_bf16 v12, v42, s12, v12\n\t"
    "v_dot2_f32_bf16 v13, v42, s13, v13\n\t"
    "v_dot2_f32_bf16 v14, v43, s12, v14\n\t"
    "v_dot2_f32_bf16 v15, v43, s13, v15\n\t"
    "s_waitcnt vmcnt(13)\n\t"
    "v_dot2_f32_bf16 v8, v44, s12, v8\n\t"
    "v_dot2_f32_bf16 v9, v44, s13, v9\n\t"
    "v_dot2_f32_bf16 v10, v45, s12, v10\n\t"
    "v_dot2_f32_bf16 v11, v45, s13, v11\n\t"
    "v_dot2_f32_bf16 v12, v46, s12, v12\n\t"
    "v_dot2_f32_bf16 v13, v46, s13, v13\n\t"
    "v_dot2_f32_bf16 v14, v47, s12, v14\n\t"
    "v_dot2_f32_bf16 v15, v47, s13, v15\n\t"
    "s_waitcnt vmcnt(12)\n\t"
    "v_dot2_f32_bf16 v8, v48, s12, v8\n\t"
    "v_dot2_f32_bf16 v9, v48, s13, v9\n\t"
    "v_dot2_f32_bf16 v10, v49, s12, v10\n\t"
    "v_dot2_f32_bf16 v11, v49, s13, v11\n\t"
    "v_dot2_f32_bf16 v12, v50, s12, v12\n\t"
    "v_dot2_f32_bf16 v13, v50, s13, v13\n\t"
    "v_dot2_f32_bf16 v14, v51, s12, v14\n\t"
    "v_dot2_f32_bf16 v15, v51, s13, v15\n\t"
    "s_waitcnt vmcnt(11)\n\t"
    "v_dot2_f32_bf16 v8, v52, s12, v8\n\t"
    "v_dot2_f32_bf16 v9, v52, s13, v9\n\t"
    "v_dot2_f32_bf16 v10, v53, s12, v10\n\t"
    "v_dot2_f32_bf16 v11, v53, s13, v11\n\t"
    "v_dot2_f32_bf16 v12, v54, s12, v12\n\t"
    "v_dot2_f32_bf16 v13, v54, s13, v13\n\t"
    "v_dot2_f32_bf16 v14, v55, s12, v14\n\t"
    "v_dot2_f32_bf16 v15, v55, s13, v15\n\t"
    "s_waitcnt vmcnt(10)\n\t"
    "v_dot2_f32_bf16 v8, v56, s12, v8\n\t"
    "v_dot2_f32_bf16 v9, v56, s13, v9\n\t"
    "v_dot2_f32_bf16 v10, v57, s12, v10\n\t"
    "v_dot2_f32_bf16 v11, v57, s13, v11\n\t"
    "v_dot2_f32_bf16 v12, v58, s12, v12\n\t"
    "v_dot2_f32_bf16 v13, v58, s13, v13\n\t"
    "v_dot2_f32_bf16 v14, v59, s12, v14\n\t"
    "v_dot2_f32_bf16 v15, v59, s13, v15\n\t"
    "s_waitcnt vmcnt(9)\n\t"
    "v_dot2_f32_bf16 v8, v60, s12, v8\n\t"
    "v_dot2_f32_bf16 v9, v60, s13, v9\n\t"
    "v_dot2_f32_bf16 v10, v61, s12, v10\n\t"
    "v_dot2_f32_bf16 v11, v61, s13, v11\n\t"
    "v_dot2_f32_bf16 v12, v62, s12, v12\n\t"
    "v_dot2_f32_bf16 v13, v62, s13, v13\n\t"
    "v_dot2_f32_bf16 v14, v63, s12, v14\n\t"
    "v_dot2_f32_bf16 v15, v63, s13, v15\n\t"
    "s_waitcnt vmcnt(8)\n\t"
    "v_dot2_f32_bf16 v8, v64, s12, v8\n\t"
    "v_dot2_f32_bf16 v9, v64, s13, v9\n\t"
    "v_dot2_f32_bf16 v10, v65, s12, v10\n\t"
    "v_dot2_f32_bf16 v11, v65, s13, v11\n\t"
    "v_dot2_f32_bf16 v12, v66, s12, v12\n\t"
    "v_dot2_f32_bf16 v13, v66, s13, v13\n\t"
    "v_dot2_f32_bf16 v14, v67, s12, v14\n\t"
    "v_dot2_f32_bf16 v15, v67, s13, v15\n\t"
    "s_waitcnt vmcnt(7)\n\t"
    "v_dot2_f32_bf16 v8, v68, s12, v8\n\t"
    "v_dot2_f32_bf16 v9, v68, s13, v9\n\t"
    "v_dot2_f32_bf16 v10, v69, s12, v10\n\t"
    "v_dot2_f32_bf16 v11, v69, s13, v11\n\t"
    "v_dot2_f32_bf16 v12, v70, s12, v12\n\t"
    "v_dot2_f32_bf16 v13, v70, s13, v13\n\t"
    "v_dot2_f32_bf16 v14, v71, s12, v14\n\t"
    "v_dot2_f32_bf16 v15, v71, s13, v15\n\t"
    "s_waitcnt vmcnt(6)\n\t"
    "v_dot2_f32_bf16 v8, v72, s12, v8\n\t"
    "v_dot2_f32_bf16 v9, v72, s13, v9\n\t"
    "v_dot2_f32_bf16 v10, v73, s12, v10\n\t"
    "v_dot2_f32_bf16 v11, v73, s13, v11\n\t"
    "v_dot2_f32_bf16 v12, v74, s12, v12\n\t"
    "v_dot2_f32_bf16 v13, v74, s13, v13\n\t"
    "v_dot2_f32_bf16 v14, v75, s12, v14\n\t"
    "v_dot2_f32_bf16 v15, v75, s13, v15\n\t"
    "s_waitcnt vmcnt(5)\n\t"
    "v_dot2_f32_bf16 v8, v76, s12, v8\n\t"
    "v_dot2_f32_bf16 v9, v76, s13, v9\n\t"
    "v_dot2_f32_bf16 v10, v77, s12, v10\n\t"
    "v_dot2_f32_bf16 v11, v77, s13, v11\n\t"
    "v_dot2_f32_bf16 v12, v78, s12, v12\n\t"
    "v_dot2_f32_bf16 v13, v78, s13, v13\n\t"
    "v_dot2_f32_bf16 v14, v79, s12, v14\n\t"
    "v_dot2_f32_bf16 v15, v79, s13, v15\n\t"
    "s_waitcnt vmcnt(4)\n\t"
    "v_dot2_f32_bf16 v8, v80, s12, v8\n\t"
    "v_dot2_f32_bf16 v9, v80, s13, v9\n\t"
    "v_dot2_f32_bf16 v10, v81, s12, v10\n\t"
    "v_dot2_f32_bf16 v11, v81, s13, v11\n\t"
    "v_dot2_f32_bf16 v12, v82, s12, v12\n\t"
    "v_dot2_f32_bf16 v13, v82, s13, v13\n\t"
    "v_dot2_f32_bf16 v14, v83, s12, v14\n\t"
    "v_dot2_f32_bf16 v15, v83, s13, v15\n\t"
    "s_waitcnt vmcnt(3)\n\t"
    "v_dot2_f32_bf16 v8, v84, s12, v8\n\t"
    "v_dot2_f32_bf16 v9, v84, s13, v9\n\t"
    "v_dot2_f32_bf16 v10, v85, s12, v10\n\t"
    "v_dot2_f32_bf16 v11, v85, s13, v11\n\t"
    "v_dot2_f32_bf16 v12, v86, s12, v12\n\t"
    "v_dot2_f32_bf16 v13, v86, s13, v13\n\t"
    "v_dot2_f32_bf16 v14, v87, s12, v14\n\t"
    "v_dot2_f32_bf16 v15, v87, s13, v15\n\t"
    "s_waitcnt vmcnt(2)\n\t"
    "v_dot2_f32_bf16 v8, v88, s12, v8\n\t"
    "v_dot2_f32_bf16 v9, v88, s13, v9\n\t"
    "v_dot2_f32_bf16 v10, v89, s12, v10\n\t"
    "v_dot2_f32_bf16 v11, v89, s13, v11\n\t"
    "v_dot2_f32_bf16 v12, v90, s12, v12\n\t"
    "v_dot2_f32_bf16 v13, v90, s13, v13\n\t"
    "v_dot2_f32_bf16 v14, v91, s12, v14\n\t"
    "v_dot2_f32_bf16 v15, v91, s13, v15\n\t"
    "s_waitcnt vmcnt(1)\n\t"
    "v_dot2_f32_bf16 v8, v92, s12, v8\n\t"
    "v_dot2_f32_bf16 v9, v92, s13, v9\n\t"
    "v_dot2_f32_bf16 v10, v93, s12, v10\n\t"
    "v_dot2_f32_bf16 v11, v93, s13, v11\n\t"
    "v_dot2_f32_bf16 v12, v94, s12, v12\n\t"
    "v_dot2_f32_bf16 v13, v94, s13, v13\n\t"
    "v_dot2_f32_bf16 v14, v95, s12, v14\n\t"
    "v_dot2_f32_bf16 v15, v95, s13, v15\n\t"
    "s_waitcnt vmcnt(0)\n\t"
    "v_dot2_f32_bf16 v8, v96, s12, v8\n\t"
    "v_dot2_f32_bf16 v9, v96, s13, v9\n\t"
    "v_dot2_f32_bf16 v10, v97, s12, v10\n\t"
    "v_dot2_f32_bf16 v11, v97, s13, v11\n\t"
    "v_dot2_f32_bf16 v12, v98, s12, v12\n\t"
    "v_dot2_f32_bf16 v13, v98, s13, v13\n\t"
    "v_dot2_f32_bf16 v14, v99, s12, v14\n\t"
    "v_dot2_f32_bf16 v15, v99, s13, v15\n\t"
    "s_nop 2\n\t"
    "v_cvt_pk_bf16_f32 v36, v8, v9\n\t"
    "v_cvt_pk_bf16_f32 v37, v10, v11\n\t"
    "v_cvt_pk_bf16_f32 v38, v12, v13\n\t"
    "v_cvt_pk_bf16_f32 v39, v14, v15\n\t"
    "s_nop 0\n\t"
    "v_permlane16_swap_b32 v36, v38\n\t"
    "v_permlane16_swap_b32 v37, v39\n\t"
    "buffer_store_dwordx4 v[36:39], v4, s[8:11], s60 offen \n\t"
    "s_cmp_lg_u32 s14, 0\n\t"
    "s_cbranch_scc1 9f\n\t"
    "s_lshl_b32 s30, s50, 7\n\t"
    "v_mov_b32 v8, s34\n\t"
    "v_mov_b32 v9, 0\n\t"
    "s_mov_b64 exec, 1\n\t"
    "buffer_store_dword v8, v9, s[24:27], s30 offen sc1\n\t"
    "s_mov_b64 exec, -1\n\t"
    "9:\n\t"
    "s_endpgm\n\t"
    "8:\n\t"
    "s_barrier\n\t"
    "s_branch 9b\n\t"
    "gqa_lfb:\n\t"
    "s_waitcnt vmcnt(0)\n\t"
    "s_lshl_b32 s35, s53, 7\n\t"
    "s_mov_b32 s68, 0\n\t"
    "s_mov_b32 s65, 0x10000\n\t"
    "s_add_u32 s67, s53, 3\n\t"
    "s_cmp_ge_u32 s67, 9\n\t"
    "s_cselect_b32 s30, 9, 0\n\t"
    "s_sub_u32 s67, s67, s30\n\t"
    "s_lshl_b32 s67, s67, 7\n\t"
    "s_add_u32 m0, s36, s65\n\t"
    "s_nop 0\n\t"
    "buffer_load_dwordx4 v9, s[12:15], s68 offen nt lds\n\t"
    "buffer_load_dwordx4 v10, s[12:15], s68 offen offset:1024 nt lds\n\t"
    "buffer_load_dwordx4 v11, s[12:15], s68 offen offset:2048 nt lds\n\t"
    "buffer_load_dwordx4 v12, s[12:15], s68 offen offset:3072 nt lds\n\t"
    "s_add_u32 s68, s68, 8192\n\t"
    "s_add_u32 s65, s65, 16384\n\t"
    "s_and_b32 s65, s65, 0x1ffff\n\t"
    "s_add_u32 m0, s36, s65\n\t"
    "s_nop 0\n\t"
    "buffer_load_dwordx4 v9, s[12:15], s68 offen nt lds\n\t"
    "buffer_load_dwordx4 v10, s[12:15], s68 offen offset:1024 nt lds\n\t"
    "buffer_load_dwordx4 v11, s[12:15], s68 offen offset:2048 nt lds\n\t"
    "buffer_load_dwordx4 v12, s[12:15], s68 offen offset:3072 nt lds\n\t"
    "s_add_u32 s68, s68, 8192\n\t"
    "s_add_u32 s65, s65, 16384\n\t"
    "s_and_b32 s65, s65, 0x1ffff\n\t"
    "s_add_u32 m0, s36, s65\n\t"
    "s_nop 0\n\t"
    "buffer_load_dwordx4 v9, s[12:15], s68 offen nt lds\n\t"
    "buffer_load_dwordx4 v10, s[12:15], s68 offen offset:1024 nt lds\n\t"
    "buffer_load_dwordx4 v11, s[12:15], s68 offen offset:2048 nt lds\n\t"
    "buffer_load_dwordx4 v12, s[12:15], s68 offen offset:3072 nt lds\n\t"
    "s_add_u32 s68, s68, 8192\n\t"
    "s_add_u32 s65, s65, 16384\n\t"
    "s_and_b32 s65, s65, 0x1ffff\n\t"
    "s_add_u32 m0, s36, s65\n\t"
    "s_nop 0\n\t"
    "buffer_load_dwordx4 v9, s[12:15], s68 offen nt lds\n\t"
    "buffer_load_dwordx4 v10, s[12:15], s68 offen offset:1024 nt lds\n\t"
    "buffer_load_dwordx4 v11, s[12:15], s68 offen offset:2048 nt lds\n\t"
    "buffer_load_dwordx4 v12, s[12:15], s68 offen offset:3072 nt lds\n\t"
    "s_add_u32 s68, s68, 8192\n\t"
    "s_add_u32 s65, s65, 16384\n\t"
    "s_and_b32 s65, s65, 0x1ffff\n\t"
    "s_add_u32 m0, s36, s65\n\t"
    "s_nop 0\n\t"
    "buffer_load_dwordx4 v9, s[12:15], s68 offen nt lds\n\t"
    "buffer_load_dwordx4 v10, s[12:15], s68 offen offset:1024 nt lds\n\t"
    "buffer_load_dwordx4 v11, s[12:15], s68 offen offset:2048 nt lds\n\t"
    "buffer_load_dwordx4 v12, s[12:15], s68 offen offset:3072 nt lds\n\t"
    "s_add_u32 s68, s68, 8192\n\t"
    "s_add_u32 s65, s65, 16384\n\t"
    "s_and_b32 s65, s65, 0x1ffff\n\t"
    "s_add_u32 m0, s36, s65\n\t"
    "s_nop 0\n\t"
    "buffer_load_dwordx4 v9, s[12:15], s68 offen nt lds\n\t"
    "buffer_load_dwordx4 v10, s[12:15], s68 offen offset:1024 nt lds\n\t"
    "buffer_load_dwordx4 v11, s[12:15], s68 offen offset:2048 nt lds\n\t"
    "buffer_load_dwordx4 v12, s[12:15], s68 offen offset:3072 nt lds\n\t"
    "s_add_u32 s68, s68, 8192\n\t"
    "s_add_u32 s65, s65, 16384\n\t"
    "s_and_b32 s65, s65, 0x1ffff\n\t"
    "s_add_u32 m0, s36, s65\n\t"
    "s_nop 0\n\t"
    "buffer_load_dwordx4 v9, s[12:15], s68 offen nt lds\n\t"
    "buffer_load_dwordx4 v10, s[12:15], s68 offen offset:1024 nt lds\n\t"
    "s_waitcnt vmcnt(0)\n\t"
    "s_barrier\n\t"
    "s_branch gqa_latt\n\t"
    : : [ka]"s"(ka), [bid]"s"(bid), [wid]"s"(wid), [lds]"s"(ldsb) : "memory","vcc","scc","s4","s5","s6","s7","s8","s9","s10","s11","s12","s13","s14","s15","s16","s17","s18","s19","s20","s21","s22","s23","s24","s25","s26","s27","s28","s29","s30","s31","s32","s33","s34","s35","s36","s37","s38","s39","s40","s41","s42","s43","s44","s45","s46","s47","s48","s49","s50","s51","s52","s53","s54","s55","s56","s57","s58","s59","s60","s61","s62","s63","s64","s65","s66","s67","s68","s69","v0","v1","v2","v3","v4","v5","v6","v7","v8","v9","v10","v11","v12","v13","v14","v15","v16","v17","v18","v19","v20","v21","v22","v23","v24","v25","v26","v27","v28","v29","v30","v31","v32","v33","v34","v35","v36","v37","v38","v39","v40","v41","v42","v43","v44","v45","v46","v47","v48","v49","v50","v51","v52","v53","v54","v55","v56","v57","v58","v59","v60","v61","v62","v63","v64","v65","v66","v67","v68","v69","v70","v71","v72","v73","v74","v75","v76","v77","v78","v79","v80","v81","v82","v83","v84","v85","v86","v87","v88","v89","v90","v91","v92","v93","v94","v95","v96","v97","v98","v99","v100","v101","v102","v103","v104","v105","v106","v107","v108","v109","v110","v111","v112","v113","v114","v115","v116","v117","v118","v119","v120","v121","v122","v123","v124","v125","v126","v127","v128","v129","v130","v131","v132","v133","v134","v135","v136","v137","v138","v139","v140","v141","v142","v143","v144","v145","v146","v147","v148","v149","v150","v151","v152","v153","v154","v155","v156","v157","v158","v159","v160","v161","v162","v163","v164","v165","v166","v167","v168","v169","v170","v171","v172","v173","v174","v175","v176","v177","v178","v179","v180","v181","v182","v183","v184","v185","v186","v187","v188","v189","v190","v191","v192","v193","v194","v195","v196","v197","v198","v199","v200","v201","v202","v203","v204","v205","v206","v207","v208","v209","v210","v211","v212","v213","v214","v215","v216","v217","v218","v219","v220","v221","v222","v223","v224","v225","v226","v227","v228","v229","v230","v231","v232","v233","v234","v235","v236","v237","v238","v239","v240","v241","v242","v243","v244","v245","v246","v247","v248","v249","v250","v251","v252","v253","v254","v255");
  }
}

extern "C" void kernel_launch(void* const* d_in, const int* in_sizes, int n_in, void* d_out, int out_size, void* d_ws, size_t ws_size, hipStream_t stream) {
  gqa_fused<<<256, 512, 0, stream>>>(d_in[0], d_in[1], d_in[2], d_in[3], d_in[4], d_out, d_ws);
}
```
